```python
import torch

def _softmax(x, axis=-1):
    return torch.softmax(x, dim=axis)
def _gelu(x):
    return torch.nn.functional.gelu(x, approximate="tanh")
def _silu(x):
    return torch.nn.functional.silu(x)
def _sigmoid(x):
    return torch.sigmoid(x)
def _layernorm(x, axis=-1, eps=1e-5):
    m = x.mean(dim=axis, keepdim=True)
    v = x.var(dim=axis, keepdim=True, unbiased=False)
    return (x - m) / torch.sqrt(v + eps)
def _scatter_add(tgt, idx, src, axis=0):
    return tgt.clone().index_add_(axis, idx, src)
def _segment_sum(src, seg, n):
    out = torch.zeros((n,) + src.shape[1:], dtype=src.dtype)
    return out.index_add_(0, seg, src)
def _pad(x, pw):
    flat = tuple(v for pair in reversed(pw) for v in pair)
    return torch.nn.functional.pad(x, flat)

kernel_name = 'parallel_sum__ssm_conv_s4__swiglu_ffn__prompt'

def setup_inputs():

    _w = (lambda a: (lambda t: t.to(torch.bfloat16) if t.is_floating_point() else t)(torch.as_tensor(a)))
    def _A_setup():
        B, L, D, N = 1, 2048, 640, 192
        A_re = -(lambda _x: torch.exp(torch.as_tensor(_x)))((lambda *s: torch.randn(*s, dtype=torch.bfloat16) * 3.2)(D, N))
        A_im = torch.pi * (lambda *s: torch.randn(*s, dtype=torch.bfloat16) * 0.2)(D, N)
        B_re = (lambda *s: torch.randn(*s, dtype=torch.bfloat16) * 0.4)(D, N)
        B_im = (lambda *s: torch.randn(*s, dtype=torch.bfloat16) * 0.4)(D, N)
        C_re = (lambda *s: torch.randn(*s, dtype=torch.bfloat16) * 0.4)(D, N)
        C_im = (lambda *s: torch.randn(*s, dtype=torch.bfloat16) * 0.4)(D, N)
        log_dt = (lambda *s: torch.randn(*s, dtype=torch.bfloat16) * 0.2)(D) * 0.5 - 4.0
        Dskip = (lambda *s: torch.randn(*s, dtype=torch.bfloat16) * 0.4)(D)
        return {
            "u": _w((lambda *s: torch.randn(*s, dtype=torch.bfloat16) * 0.05)(B, L, D)),
            "A_re": _w(A_re),
            "A_im": _w(A_im),
            "B_re": _w(B_re),
            "B_im": _w(B_im),
            "C_re": _w(C_re),
            "C_im": _w(C_im),
            "log_dt": _w(log_dt),
            "Dskip": _w(Dskip),
        }
    _A_in = _A_setup()

    def _B_setup():
        B, S, D, F = 1, 2048, 640, 1536
        return {
            "x": _w((lambda *s: torch.randn(*s, dtype=torch.bfloat16) * 0.05)(B, S, D)),
            "w_gate": _w((lambda *s: torch.randn(*s, dtype=torch.bfloat16) * 0.05)(D, F)),
            "w_up": _w((lambda *s: torch.randn(*s, dtype=torch.bfloat16) * 0.05)(D, F)),
            "w_down": _w((lambda *s: torch.randn(*s, dtype=torch.bfloat16) * 0.05)(F, D)),
        }
    _B_in = _B_setup()

    return {
        "A_u": _A_in["u"],
        "A_A_re": _A_in["A_re"],
        "A_A_im": _A_in["A_im"],
        "A_B_re": _A_in["B_re"],
        "A_B_im": _A_in["B_im"],
        "A_C_re": _A_in["C_re"],
        "A_C_im": _A_in["C_im"],
        "A_log_dt": _A_in["log_dt"],
        "A_Dskip": _A_in["Dskip"],
        "B_x": _B_in["x"],
        "B_w_gate": _B_in["w_gate"],
        "B_w_up": _B_in["w_up"],
        "B_w_down": _B_in["w_down"],
    }

def reference(A_u, A_A_re, A_A_im, A_B_re, A_B_im, A_C_re, A_C_im, A_log_dt, A_Dskip, B_x, B_w_gate, B_w_up, B_w_down):
    def _A_ref(u, A_re, A_im, B_re, B_im, C_re, C_im, log_dt, Dskip):
        Bsz, L, D = u.shape
        dt = torch.exp(log_dt)[:, None]
        A = A_re + 1j * A_im
        Bc = B_re + 1j * B_im
        Cc = C_re + 1j * C_im
        logdA = dt * A
        dB = dt * Bc
        l = (lambda x, dt: x.to(dt))(torch.arange(L), A_re.dtype)
        P = torch.exp(logdA[:, :, None] * l[None, None, :])
        K = torch.einsum("dn,dnl,dn->dl", Cc, P, dB).real
        L2 = 2 * L
        Kp = _pad(K, ((0, 0), (0, L)))
        up = (lambda x, *d: x.permute(*d))(u, 0, 2, 1)
        up = _pad(up, ((0, 0), (0, 0), (0, L)))
        Kf = (lambda x, axes=(-2,-1): torch.fft.rfft2(x.float(), dim=axes))(Kp[:, None, :], axes=(-2, -1))
        Uf = (lambda x, axes=(-2,-1): torch.fft.rfft2(x.float(), dim=axes))(up[:, :, None, :], axes=(-2, -1))
        Yf = Uf * Kf[None, :, :, :]
        y = (lambda x, s, axes=(-2,-1): torch.fft.irfft2(x, s=s, dim=axes).to(torch.bfloat16))(Yf, (1, L2), axes=(-2, -1))[:, :, 0, :L]
        y = (lambda x, *d: x.permute(*d))(y, 0, 2, 1) + u * Dskip[None, None, :]
        return y

    def _B_ref(x, w_gate, w_up, w_down):
        g = _silu(x @ w_gate)
        u = x @ w_up
        return (g * u) @ w_down

    x = A_u
    xn = _layernorm(x, axis=-1)
    return x + _A_ref(xn, A_A_re, A_A_im, A_B_re, A_B_im, A_C_re, A_C_im, A_log_dt, A_Dskip) + _B_ref(xn, B_w_gate, B_w_up, B_w_down)
```

```cpp
#include <hip/hip_runtime.h>
#include <hip/hip_bf16.h>

typedef unsigned u32;
typedef unsigned long long u64;
typedef unsigned short u16;
typedef u32 u32x2 __attribute__((ext_vector_type(2)));
typedef u32 u32x4 __attribute__((ext_vector_type(4)));
typedef float f32x2 __attribute__((ext_vector_type(2)));
typedef float f32x4 __attribute__((ext_vector_type(4)));
typedef float f32x16 __attribute__((ext_vector_type(16)));
typedef __bf16 bf16x2 __attribute__((ext_vector_type(2)));
typedef __bf16 bf16x4 __attribute__((ext_vector_type(4)));
typedef __bf16 bf16x8 __attribute__((ext_vector_type(8)));
#define LAS __attribute__((address_space(3)))
#define GAS __attribute__((address_space(1)))
#define DI __device__ __forceinline__

constexpr u32 WS_EPOCH = 0, WS_F1 = 4096, WS_F3 = 8192, WS_F2 = 16384, WS_STAT = 65536, WS_XNT = 131072;
constexpr u32 WS_WDF = WS_XNT + 2621440, WS_H = WS_WDF + 1966080, WS_YT = WS_H + 6291456, WS_END = WS_YT + 2621440;
constexpr u32 GOFF = 16384, UOFF = 16384 + 12288 + 32, SLOT = UOFF + 12288;
constexpr u32 LDS_S = 3 * SLOT, S_SZ = 8704, LDS_UU = LDS_S + 3 * S_SZ, U_SZ = 4576, LDS_MISC = LDS_UU + 3 * U_SZ;
constexpr u32 LDS_TOTAL = 163840;
constexpr u32 KC_OFF = 4160;
constexpr u32 PROW = 176, PWAVE = 16 * PROW, PTILE = 8 * PWAVE, LDS_Y = 4 * PTILE, YROW = 144;
constexpr u32 SPIN_CAP = 1u << 16;
constexpr int SC1 = 16;

#define STAMP(k) do { } while (0)
#define BARS() BAR()

#if defined(__HIP_DEVICE_COMPILE__)
#define SB() __builtin_amdgcn_sched_barrier(0)
#define BAR() do { SB(); asm volatile("s_waitcnt lgkmcnt(0)\n\ts_barrier" ::: "memory"); SB(); } while (0)
#define DRAIN() asm volatile("s_waitcnt vmcnt(0)" ::: "memory")

DI float bf_lo(u32 v) { return __builtin_bit_cast(float, v << 16); }
DI float bf_hi(u32 v) { return __builtin_bit_cast(float, v & 0xffff0000u); }
DI u32 pk_bf(float a, float b) { f32x2 v = {a, b}; return __builtin_bit_cast(u32, __builtin_convertvector(v, bf16x2)); }
DI u32 pk_bf(f32x2 v) { return __builtin_bit_cast(u32, __builtin_convertvector(v, bf16x2)); }
DI float rnd_bf(float a) { return bf_lo(pk_bf(a, a)); }
DI float as_f(u32 v) { return __builtin_bit_cast(float, v); }
DI u32 as_u(float v) { return __builtin_bit_cast(u32, v); }
DI bf16x2 as_bf2(u32 v) { return __builtin_bit_cast(bf16x2, v); }
DI bf16x8 as_frag(u32x4 v) { return __builtin_bit_cast(bf16x8, v); }
DI f32x2 cmul(f32x2 x, f32x2 y) {
    f32x2 xr = {x.x, x.x}, xi = {x.y, x.y}, ys = {-y.y, y.x};
    return __builtin_elementwise_fma(xi, ys, xr * y);
}
DI f32x2 cexp_step(float a2, float b2, float t) {
    float mag = __builtin_amdgcn_exp2f(a2 * t), ph = __builtin_amdgcn_fractf(b2 * t);
    f32x2 r = {__builtin_amdgcn_cosf(ph), __builtin_amdgcn_sinf(ph)};
    return r * mag;
}
template <class T> DI T lds_ld(LAS char* p) { return *(LAS T*)p; }
template <class T> DI void lds_st(LAS char* p, T v) { *(LAS T*)p = v; }
DI u32x2 lds_tr(LAS char* p) { return __builtin_bit_cast(u32x2, __builtin_amdgcn_ds_read_tr16_b64_v4bf16((LAS bf16x4*)p)); }
DI u32x4 lds_tr2(LAS char* p, u32 step) { u32x2 a = lds_tr(p), b = lds_tr(p + step); return u32x4{a.x, a.y, b.x, b.y}; }
DI u32x4 make_srd(const void* p, u32 bytes) { u64 a = (u64)p; return u32x4{(u32)a, (u32)(a >> 32) & 0xffffu, bytes, 0x00020000u}; }
DI void dma16(u32 lds_addr, u32 voff, u32x4 srd, u32 soff) {
    asm volatile("s_mov_b32 m0, %0\n\ts_nop 0\n\tbuffer_load_dwordx4 %1, %2, %3 offen lds" ::"s"(lds_addr), "v"(voff), "s"(srd), "s"(soff) : "memory");
}
DI u64 ld_flag(char* ws, u32 off) { return __hip_atomic_load((GAS u64*)(ws + off), __ATOMIC_RELAXED, __HIP_MEMORY_SCOPE_AGENT); }
DI void st_flag(char* ws, u32 off, u64 v) { __hip_atomic_store((GAS u64*)(ws + off), v, __ATOMIC_RELAXED, __HIP_MEMORY_SCOPE_AGENT); }
template <int N> DI void wait_flags(char* ws, u32 off, u32 stride, u64 tag) {
    for (u32 spin = 0; spin < SPIN_CAP; ++spin) {
        bool ok = true;
#pragma unroll
        for (int i = 0; i < N; ++i) ok &= ld_flag(ws, off + i * stride) == tag;
        if (__all(ok)) break;
        __builtin_amdgcn_s_sleep(2);
    }
    __builtin_amdgcn_fence(__ATOMIC_ACQUIRE, "wavefront");
    asm volatile("" ::: "memory");
}
DI void signal_last(LAS char* cnt, u32 n, char* ws, u32 off, u64 tag, int lane) {
    DRAIN();
    u32 old = 0;
    if (lane == 0) old = __hip_atomic_fetch_add((LAS u32*)cnt, 1u, __ATOMIC_RELAXED, __HIP_MEMORY_SCOPE_WORKGROUP);
    old = __builtin_amdgcn_readfirstlane(old);
    if (old == n - 1 && lane == 0) st_flag(ws, off, tag);
}
template <int CTRL> DI float dpp_add(float v) {
    int t = __builtin_amdgcn_update_dpp(0, __builtin_bit_cast(int, v), CTRL, 0xf, 0xf, true);
    return v + __builtin_bit_cast(float, t);
}
DI void reduce8(const float (&v)[8], float (&o)[2]) {
    float s[4];
#pragma unroll
    for (int i = 0; i < 4; ++i) {
        auto r = __builtin_amdgcn_permlane32_swap(as_u(v[i]), as_u(v[i + 4]), false, false);
        s[i] = as_f(r[0]) + as_f(r[1]);
    }
#pragma unroll
    for (int i = 0; i < 2; ++i) {
        auto r = __builtin_amdgcn_permlane16_swap(as_u(s[i]), as_u(s[i + 2]), false, false);
        float t = as_f(r[0]) + as_f(r[1]);
        t = dpp_add<0x128>(t); t = dpp_add<0x124>(t); t = dpp_add<0x122>(t); t = dpp_add<0x121>(t);
        o[i] = t;
    }
}
DI float lane_bcast(float v, int l) { return __builtin_bit_cast(float, __builtin_amdgcn_readlane(__builtin_bit_cast(int, v), l)); }
#endif

__global__ void __launch_bounds__(512, 2)
fused_kernel(const u16* __restrict__ x, const u16* __restrict__ Are, const u16* __restrict__ Aim, const u16* __restrict__ Bre,
             const u16* __restrict__ Bim, const u16* __restrict__ Cre, const u16* __restrict__ Cim, const u16* __restrict__ logdt,
             const u16* __restrict__ Dskip, const u16* __restrict__ Wg, const u16* __restrict__ Wu, const u16* __restrict__ Wd,
             u16* __restrict__ out, char* ws) {
#if defined(__HIP_DEVICE_COMPILE__)
    extern __shared__ __attribute__((aligned(16))) char smem_[];
    LAS char* const smem = (LAS char*)smem_;
    const u32 lds0 = (u32)(size_t)smem;
    const int b = blockIdx.x, xcd = b & 7, wi = b >> 3;
    const int w = __builtin_amdgcn_readfirstlane(threadIdx.x >> 6), lane = threadIdx.x & 63;
    const __amdgpu_buffer_rsrc_t wsr = __builtin_amdgcn_make_buffer_rsrc(ws, 0, WS_END, 0x00020000);
    const int panel = 8 * (xcd & 1) + (wi & 7), slice = 4 * (xcd >> 1) + (wi >> 3);
    const int sb = 32 * xcd + wi, ch0 = (5 * sb + 1) >> 1, nch = 3 - (sb & 1);
    const int ttile = 4 * xcd + (wi & 3), ctile = wi >> 2;
    STAMP(0);

    u32x4 fb[6][5];
    auto load_fb = [&]() {
#pragma unroll
        for (int k = 0; k < 6; ++k)
#pragma unroll
            for (int j = 0; j < 5; ++j) fb[k][j] = __builtin_amdgcn_raw_buffer_load_b128(wsr, WS_WDF + lane * 16 + j * 1024, ((ctile * 48 + 6 * w + k) * 5) * 1024, SC1);
    };
    u64 tag;

    if (w < 4) {
        __builtin_amdgcn_s_setprio(2);
        f32x16 acc[3][2];
        float cs[3] = {0.f, 0.f, 0.f};
#pragma unroll
        for (int m = 0; m < 3; ++m)
#pragma unroll
            for (int n = 0; n < 2; ++n)
#pragma unroll
                for (int r = 0; r < 16; ++r) acc[m][n][r] = 0.f;
        const int mi = w & 1, ni = w >> 1, h = lane >> 5, r32 = lane & 31;
        const u32x4 xs = make_srd(x, 2048 * 1280), gs = make_srd(Wg, 640 * 3072), us = make_srd(Wu, 640 * 3072);
        const u32 vx = (8 * w + (lane >> 3)) * 1280 + (((lane & 7) ^ ((4 * w + (lane >> 4)) & 7)) << 4);
        u32 vw[3];
#pragma unroll
        for (int j = 0; j < 3; ++j) vw[j] = ((64 * j + lane) / 12) * 3072 + ((64 * j + lane) % 12) * 16;
        const u32 sx0 = panel * 128 * 1280, sw0 = 16 * w * 3072 + slice * 192;
        const int rot = (wi * 3 + xcd) % 10;
        auto piece = [&](int i, u32 slot, u32 kt) {
            if (i < 4) dma16(lds0 + slot + (w + 4 * i) * 1024, vx, xs, sx0 + i * 32 * 1280 + kt * 128);
            else if (i < 7) dma16(lds0 + slot + GOFF + (3 * w + i - 4) * 1024, vw[i - 4], gs, sw0 + kt * 64 * 3072);
            else dma16(lds0 + slot + UOFF + (3 * w + i - 7) * 1024, vw[i - 7], us, sw0 + kt * 64 * 3072);
        };
        u32 kt = rot;
#pragma unroll
        for (int i = 0; i < 10; ++i) piece(i, 0, kt);
        kt = kt == 9 ? 0 : kt + 1;
#pragma unroll
        for (int i = 0; i < 10; ++i) piece(i, SLOT, kt);
        kt = kt == 9 ? 0 : kt + 1;

        const u32 hz = h ^ ((r32 >> 1) & 7);
        u32 ax[4];
#pragma unroll
        for (int s = 0; s < 4; ++s) ax[s] = (64 * ni + r32) * 128 + (((2 * s) ^ hz) << 4);
        const u32 aw = (((lane >> 4) & 1) ? UOFF : GOFF) + (8 * h + ((lane & 15) >> 2)) * 192 + mi * 96 + (lane & 3) * 8;
        u32x4 fw[2][3], fx[2][2];
        const bf16x2 ones = {(__bf16)1.0f, (__bf16)1.0f};
        auto rd = [&](int set, u32 slot, int s) {
#pragma unroll
            for (int m = 0; m < 3; ++m) fw[set][m] = lds_tr2(smem + slot + aw + s * 3072 + m * 32, 768);
#pragma unroll
            for (int n = 0; n < 2; ++n) fx[set][n] = lds_ld<u32x4>(smem + slot + ax[s] + n * 4096);
        };
        auto mm = [&](int set, int m, int n) {
            acc[m][n] = __builtin_amdgcn_mfma_f32_32x32x16_bf16(as_frag(fw[set][m]), as_frag(fx[set][n]), acc[m][n], 0, 0, 0);
        };
        auto dots = [&](int set, int m) {
#pragma unroll
            for (int e = 0; e < 4; ++e) cs[m] = __builtin_amdgcn_fdot2_f32_bf16(as_bf2(fw[set][m][e]), ones, cs[m], false);
        };
        auto group = [&](int set, bool issue, int p0, int np, u32 nslot, u32 nkt) {
#pragma unroll
            for (int m = 0; m < 3; ++m) {
                mm(set, m, 0); SB();
                if (m < np && issue) piece(p0 + m, nslot, nkt);
                SB();
                mm(set, m, 1); SB();
                dots(set, m); SB();
            }
        };
        u32 slot = 0, nslot = 2 * SLOT;
        auto tile = [&](bool first, bool issue) {
            BARS();
            rd(0, slot, 0); SB();
            if (!first) group(1, issue, 0, 3, nslot, kt);
            else { piece(0, nslot, kt); piece(1, nslot, kt); piece(2, nslot, kt); }
            rd(1, slot, 1); SB();
            group(0, issue, 3, 3, nslot, kt);
            rd(0, slot, 2); SB();
            group(1, issue, 6, 2, nslot, kt);
            rd(1, slot, 3); SB();
            group(0, issue, 8, 2, nslot, kt);
            nslot = slot;
            slot = slot == 2 * SLOT ? 0 : slot + SLOT;
            kt = kt == 9 ? 0 : kt + 1;
        };
        asm volatile("s_waitcnt vmcnt(10)" ::: "memory");
        tile(true, true);
#pragma unroll 1
        for (int t = 1; t < 8; ++t) {
            asm volatile("s_waitcnt vmcnt(10)" ::: "memory");
            tile(false, true);
        }
#pragma unroll 1
        for (int t = 8; t < 10; ++t) {
            if (t == 8) asm volatile("s_waitcnt vmcnt(10)" ::: "memory");
            else DRAIN();
            tile(false, false);
        }
        group(1, false, 0, 0, 0, 0);
        __builtin_amdgcn_s_setprio(0);
        BARS();
        tag = lds_ld<u64>(smem + LDS_MISC);
        STAMP(12);
        u32x2 stat2;
#pragma unroll
        for (int n = 0; n < 2; ++n) stat2[n] = __builtin_amdgcn_raw_buffer_load_b32(wsr, WS_STAT + (panel * 128 + 64 * ni + 32 * n + r32) * 4, 0, SC1);
        load_fb();
        u32x4 ca[3];
#pragma unroll
        for (int m = 0; m < 3; ++m) {
            const float c0 = rnd_bf(cs[m]), c1 = rnd_bf(cs[m] - c0), c2 = cs[m] - c0 - c1;
            ca[m] = u32x4{pk_bf(c0, c1), pk_bf(c2, 0.f), 0, 0};
        }
#pragma unroll
        for (int n = 0; n < 2; ++n) {
            const u32 nm = (stat2[n] & 0xffffu) ^ 0x8000u;
            const u32x4 cb = {nm | (nm << 16), nm, 0, 0};
            const float sd = bf_hi(stat2[n]), k1 = -1.44269504089f / sd, k2 = sd * sd;
            const int tok = panel * 128 + 64 * ni + 32 * n + (lane & 31);
#pragma unroll
            for (int m = 0; m < 3; ++m) {
                const f32x16 a = __builtin_amdgcn_mfma_f32_32x32x16_bf16(as_frag(ca[m]), as_frag(cb), acc[m][n], 0, 0, 0);
                u32x4 hv;
#pragma unroll
                for (int e = 0; e < 4; ++e) {
                    const f32x2 g = {a[2 * e], a[2 * e + 1]}, u = {a[8 + 2 * e], a[9 + 2 * e]};
                    const f32x2 t = g * k1;
                    const f32x2 ex = {__builtin_amdgcn_exp2f(t.x), __builtin_amdgcn_exp2f(t.y)};
                    const f32x2 d = ex * k2 + k2;
                    const f32x2 rc = {__builtin_amdgcn_rcpf(d.x), __builtin_amdgcn_rcpf(d.y)};
                    hv[e] = pk_bf(g * u * rc);
                }
                const int G = 6 * slice + 3 * mi + m;
                __builtin_amdgcn_raw_buffer_store_b128(hv, wsr, WS_H + ((tok >> 4) * 48 * 64 + (tok & 15) + 16 * h + 32 * G) * 16, 0, SC1);
            }
        }
        STAMP(13);
        signal_last(smem + LDS_MISC + 8, 4, ws, WS_F2 + (panel * 16 + slice) * 128, tag, lane);
    } else {
    if (w < 7) {
        if (w - 4 < nch) {
            const int ch = ch0 + w - 4;
            LAS char* const S = smem + LDS_S + (w - 4) * S_SZ;
            const float dt = rnd_bf(expf(bf_lo(logdt[ch])));
            u32 p[6];
            auto ldp = [&](int pass) {
                const int o = ch * 192 + pass * 64 + lane;
                p[0] = Are[o]; p[1] = Aim[o]; p[2] = Bre[o]; p[3] = Bim[o]; p[4] = Cre[o]; p[5] = Cim[o];
            };
            ldp(0);
            f32x16 K;
#pragma unroll
            for (int r = 0; r < 16; ++r) K[r] = 0.f;
            BARS();
            LAS char* const tw = S + lane * 4;
            LAS char* const tr = S + (lane & 31) * 272 + (lane >> 5) * 16;
#pragma unroll 1
            for (int pass = 0; pass < 3; ++pass) {
                const float a2 = dt * bf_lo(p[0]) * 1.44269504089f, b2 = dt * bf_lo(p[1]) * 0.15915494309f;
                const f32x2 dB = {dt * bf_lo(p[2]), -dt * bf_lo(p[3])}, Cc = {bf_lo(p[4]), -bf_lo(p[5])};
                if (pass < 2) ldp(pass + 1);
                const f32x2 W = cmul(Cc, dB);
                const f32x2 e1 = cexp_step(a2, -b2, 1.f), e256 = cexp_step(a2, -b2, 256.f), e8 = cexp_step(a2, b2, 8.f);
                const f32x2 e2 = cmul(e1, e1), e4 = cmul(e2, e2);
                f32x2 r = W, q = cmul(W, e256);
                lds_st<u32>(tw, pk_bf(r));
#pragma unroll
                for (int i = 1; i < 8; ++i) { r = cmul(r, e1); lds_st<u32>(tw + i * 272, pk_bf(r)); }
                r = q;
                lds_st<u32>(tw + 8 * 272, pk_bf(r));
#pragma unroll
                for (int i = 9; i < 12; ++i) { r = cmul(r, e2); lds_st<u32>(tw + i * 272, pk_bf(r)); }
#pragma unroll
                for (int i = 0; i < 2; ++i) {
                    q = cmul(q, e256);
                    lds_st<u32>(tw + (12 + 2 * i) * 272, pk_bf(q));
                    lds_st<u32>(tw + (13 + 2 * i) * 272, pk_bf(cmul(q, e4)));
                }
#pragma unroll
                for (int i = 16; i < 21; ++i) { q = cmul(q, e256); lds_st<u32>(tw + i * 272, pk_bf(q)); }
                BARS();
                u32x4 fa[8];
#pragma unroll
                for (int s = 0; s < 8; ++s) fa[s] = lds_ld<u32x4>(tr + s * 32);
                const f32x2 e16 = cmul(e8, e8);
                f32x2 c0 = {1.f, 0.f}, c1 = e8;
                lds_st<u32>(tw, pk_bf(c0));
                lds_st<u32>(tw + 272, pk_bf(c1));
                c0 = e16;
#pragma unroll
                for (int i = 2; i < 32; i += 2) {
                    if (i > 2) c0 = cmul(c0, e16);
                    c1 = cmul(c1, e16);
                    lds_st<u32>(tw + i * 272, pk_bf(c0));
                    lds_st<u32>(tw + (i + 1) * 272, pk_bf(c1));
                    if (i == 18) BARS();
                }
#pragma unroll
                for (int s = 0; s < 8; ++s)
                    K = __builtin_amdgcn_mfma_f32_32x32x16_bf16(as_frag(fa[s]), as_frag(lds_ld<u32x4>(tr + s * 32)), K, 0, 0, 0);
                BARS();
            }
            const int j = lane & 31, h = lane >> 5;
            if (lane == 0) K[0] += bf_lo(Dskip[ch]);
            LAS char* const C = S + KC_OFF;
            lds_st<f32x4>(C + (8 * j + 4 * h) * 4, f32x4{K[0], K[1], K[2], K[3]});
            lds_st<f32x2>(C + (h ? 384 + 2 * j : 256 + 4 * j) * 4, f32x2{K[4], K[5]});
            lds_st<f32x2>(C + (h ? 448 + 2 * j : 258 + 4 * j) * 4, f32x2{K[6], K[7]});
            lds_st<float>(C + (h ? 640 + j : 512 + j) * 4, K[8]);
#pragma unroll
            for (int i = 1; i < 4; ++i) lds_st<float>(C + (h ? 672 + j : 512 + 32 * i + j) * 4, K[8 + i]);
            {
                LAS char* const src = C + (h ? 256 + 4 * j : 8 * j) * 4;
                const f32x4 a = lds_ld<f32x4>(src), c = lds_ld<f32x4>(src + 16);
                lds_st<u32x4>(S + 32 + lane * 16, u32x4{pk_bf(a[0], h ? a[0] : a[1]), pk_bf(h ? a[1] : a[2], h ? a[2] : a[3]),
                                                         pk_bf(h ? a[2] : c[0], h ? a[2] : c[1]), pk_bf(h ? a[3] : c[2], h ? c[0] : c[3])});
            }
            {
                LAS char* const src = C + (384 + 2 * lane) * 4;
                const f32x2 a = lds_ld<f32x2>(src);
                const float c = lds_ld<float>(src + 8);
                lds_st<u32x4>(S + 32 + (64 + lane) * 16, u32x4{pk_bf(a.x, a.x), pk_bf(a.x, a.y), pk_bf(a.y, a.y), pk_bf(c, c)});
            }
#pragma unroll
            for (int i = 0; i < 2; ++i) {
                LAS char* const src = C + (512 + 64 * i + lane) * 4;
                const float a = lds_ld<float>(src), c = lds_ld<float>(src + 4);
                lds_st<u32x4>(S + 32 + (128 + 64 * i + lane) * 16, u32x4{pk_bf(a, a), pk_bf(a, a), pk_bf((lane & 1) ? c : a, c), pk_bf(c, c)});
            }
            if (lane < 2) lds_st<u32x4>(S + lane * 16, u32x4{0, 0, 0, 0});
        } else {
#pragma unroll 1
            for (int t = 0; t < 10; ++t) BARS();
        }
    } else {
        const u64 tag0 = ld_flag(ws, WS_EPOCH) + 1;
        const int lb = 128 * (xcd & 1) + 32 * (xcd >> 1) + wi;
        u32 xr[8][5];
#pragma unroll
        for (int r = 0; r < 8; ++r)
#pragma unroll
            for (int q = 0; q < 5; ++q) xr[r][q] = *(const u32*)(x + (8 * lb + r) * 640 + q * 128 + lane * 2);
        const int wks = b / 5, wnb = b % 5;
        u32x4 wd[8];
        if (b < 240) {
#pragma unroll
            for (int pc = 0; pc < 8; ++pc) wd[pc] = *(const u32x4*)(Wd + (32 * wks + 4 * pc + (lane >> 4)) * 640 + wnb * 128 + (lane & 15) * 8);
        }
        if (lane == 0) {
            lds_st<u64>(smem + LDS_MISC, tag0);
            lds_st<u32x2>(smem + LDS_MISC + 8, u32x2{0, 0});
        }
        BARS();
        float v[8], o[2], mean[8];
#pragma unroll
        for (int r = 0; r < 8; ++r) {
            float s = 0.f;
#pragma unroll
            for (int q = 0; q < 5; ++q) s += bf_lo(xr[r][q]) + bf_hi(xr[r][q]);
            v[r] = s;
        }
        reduce8(v, o);
        const float mu[2] = {o[0] * (1.f / 640.f), o[1] * (1.f / 640.f)};
#pragma unroll
        for (int r = 0; r < 8; ++r) mean[r] = lane_bcast(mu[r & 1], 16 * (r >> 1));
        BARS();
#pragma unroll
        for (int r = 0; r < 8; ++r) {
            float s = 0.f;
#pragma unroll
            for (int q = 0; q < 5; ++q) {
                const float a = bf_lo(xr[r][q]) - mean[r], c = bf_hi(xr[r][q]) - mean[r];
                s = fmaf(a, a, s); s = fmaf(c, c, s);
            }
            v[r] = s;
        }
        reduce8(v, o);
        float mb[2], rs[2];
        u32x2 stat;
#pragma unroll
        for (int i = 0; i < 2; ++i) {
            mb[i] = rnd_bf(mu[i]);
            const float sd = rnd_bf(sqrtf(rnd_bf(rnd_bf(o[i] * (1.f / 640.f)) + 1e-5f)));
            rs[i] = 1.f / sd;
            stat[i] = pk_bf(mb[i], sd);
        }
        if ((lane & 15) == 0) __builtin_amdgcn_raw_buffer_store_b64(stat, wsr, WS_STAT + (8 * lb + (lane >> 3)) * 4, 0, SC1);
        BARS();
#pragma unroll
        for (int q = 0; q < 5; ++q) {
            float lo[8], hi[8];
#pragma unroll
            for (int r = 0; r < 8; ++r) {
                const float m = lane_bcast(mb[r & 1], 16 * (r >> 1)), k = lane_bcast(rs[r & 1], 16 * (r >> 1));
                const u32 d = pk_bf(bf_lo(xr[r][q]) - m, bf_hi(xr[r][q]) - m);
                lo[r] = bf_lo(d) * k; hi[r] = bf_hi(d) * k;
            }
            const u32 off = WS_XNT + (lb * 640 + q * 128 + lane * 2) * 16;
            __builtin_amdgcn_raw_buffer_store_b128(u32x4{pk_bf(lo[7], lo[6]), pk_bf(lo[5], lo[4]), pk_bf(lo[3], lo[2]), pk_bf(lo[1], lo[0])}, wsr, off, 0, SC1);
            __builtin_amdgcn_raw_buffer_store_b128(u32x4{pk_bf(hi[7], hi[6]), pk_bf(hi[5], hi[4]), pk_bf(hi[3], hi[2]), pk_bf(hi[1], hi[0])}, wsr, off + 16, 0, SC1);
            if (q == 2) BARS();
        }
        if (b < 240) {
            LAS char* const st = smem + LDS_UU;
#pragma unroll
            for (int pc = 0; pc < 8; ++pc) lds_st<u32x4>(st + pc * 1024 + lane * 16, wd[pc]);
            const int g = lane >> 4;
            LAS char* const src = st + (16 * (g >> 1) + 4 * (g & 1) + ((lane & 15) >> 2)) * 256 + (lane & 3) * 8;
#pragma unroll
            for (int c = 0; c < 8; ++c) {
                const int ct = 8 * wnb + c;
                __builtin_amdgcn_raw_buffer_store_b128(lds_tr2(src + c * 32, 2048), wsr, WS_WDF + lane * 16 + (((ct / 5) * 48 + wks) * 5 + ct % 5) * 1024, 0, SC1);
            }
        }
        BARS();
        BARS();
        DRAIN();
        if (lane == 0) st_flag(ws, WS_F1 + b * 8, tag0);
        BARS();
        BARS();
        wait_flags<4>(ws, WS_F1 + lane * 8, 512, tag0);
        if (b == 0 && lane == 0) st_flag(ws, WS_EPOCH, tag0);
        BARS();
        u32x4 ug[12];
#pragma unroll
        for (int q = 0; q < 12; ++q) {
            const u32 n = 64 * q + lane, blk = n / 3, c = n % 3;
            ug[q] = __builtin_amdgcn_raw_buffer_load_b128(wsr, WS_XNT + (blk * 640 + ch0 + (c < nch ? c : 0)) * 16, 0, SC1);
        }
        BARS();
#pragma unroll
        for (int q = 0; q < 12; ++q) {
            const u32 n = 64 * q + lane, blk = n / 3, c = n % 3;
            lds_st<u32x4>(smem + LDS_UU + c * U_SZ + (255 - blk) * 16, ug[q]);
        }
        if (lane < 30) {
#pragma unroll
            for (int c = 0; c < 3; ++c) lds_st<u32x4>(smem + LDS_UU + c * U_SZ + 4096 + lane * 16, u32x4{0, 0, 0, 0});
        }
    }
    BARS();
    tag = lds_ld<u64>(smem + LDS_MISC);
    STAMP(12);
    load_fb();
    if (w - 4 < nch) {
        const int a = lane & 15, g = lane >> 4;
        LAS char* kp = smem + LDS_S + (w - 4) * S_SZ + ((8 * g + a + 1) >> 1) * 4;
        LAS char* up = smem + LDS_UU + (w - 4) * U_SZ + (2032 - 16 * a + 8 * g) * 2 - 7 * 512;
        const u32 sh = ((a + 1) & 1) * 16;
        f32x4 y[8];
#pragma unroll
        for (int t = 0; t < 8; ++t) y[t] = f32x4{0.f, 0.f, 0.f, 0.f};
#pragma unroll 1
        for (int rho = 0; rho < 8; ++rho) {
            u32x4 uf[8];
#pragma unroll
            for (int i = 0; i < 8; ++i) {
                uf[i] = lds_ld<u32x4>(up + (7 - i) * 512);
                LAS char* const k = kp + (7 - i) * 512;
                const u32 d0 = lds_ld<u32>(k), d1 = lds_ld<u32>(k + 4), d2 = lds_ld<u32>(k + 8), d3 = lds_ld<u32>(k + 12), d4 = lds_ld<u32>(k + 16);
                const u32x4 kf = {__builtin_amdgcn_alignbit(d1, d0, sh), __builtin_amdgcn_alignbit(d2, d1, sh),
                                  __builtin_amdgcn_alignbit(d3, d2, sh), __builtin_amdgcn_alignbit(d4, d3, sh)};
#pragma unroll
                for (int tau = 0; tau <= i; ++tau)
                    y[7 - i + tau] = __builtin_amdgcn_mfma_f32_16x16x32_bf16(as_frag(kf), as_frag(uf[tau]), y[7 - i + tau], 0, 0, 0);
            }
            kp += 64; up += 64;
        }
        const u32 off = WS_YT + (ch0 + w - 4) * 4096 + (16 * a + 4 * g) * 2;
#pragma unroll
        for (int t = 0; t < 8; ++t)
            __builtin_amdgcn_raw_buffer_store_b64(u32x2{pk_bf(y[t][0], y[t][1]), pk_bf(y[t][2], y[t][3])}, wsr, off + t * 512, 0, SC1);
        STAMP(13);
        signal_last(smem + LDS_MISC + 12, nch, ws, WS_F3 + sb * 8, tag, lane);
    }
    }
    STAMP(14);

    wait_flags<1>(ws, WS_F2 + ((ttile >> 1) * 16 + 2 * w + (lane & 1)) * 128, 0, tag);
    STAMP(15);
    {
        u32x4 fh[4][6];
#pragma unroll
        for (int m = 0; m < 4; ++m)
#pragma unroll
            for (int k = 0; k < 6; ++k) fh[m][k] = __builtin_amdgcn_raw_buffer_load_b128(wsr, WS_H + lane * 16 + (k & 3) * 1024, ((4 * ttile + m) * 48 + 6 * w + (k & 4)) * 1024, SC1);
        LAS char* const pw = smem + w * PWAVE + (lane & 15) * PROW + (lane >> 4) * 8;
#pragma unroll
        for (int m = 0; m < 4; ++m) {
            f32x4 d[5];
#pragma unroll
            for (int j = 0; j < 5; ++j) d[j] = f32x4{0.f, 0.f, 0.f, 0.f};
#pragma unroll
            for (int k = 0; k < 6; ++k)
#pragma unroll
                for (int j = 0; j < 5; ++j) d[j] = __builtin_amdgcn_mfma_f32_16x16x32_bf16(as_frag(fb[k][j]), as_frag(fh[m][k]), d[j], 0, 0, 0);
#pragma unroll
            for (int j = 0; j < 5; ++j) lds_st<u32x2>(pw + m * PTILE + j * 32, u32x2{pk_bf(d[j][0], d[j][1]), pk_bf(d[j][2], d[j][3])});
        }
    }
    STAMP(16);
    u32x2 xv[3];
#pragma unroll
    for (int i = 0; i < 3; ++i) {
        const u32 it = min(threadIdx.x + 512 * i, 1279u);
        xv[i] = *(const u32x2*)(x + (64 * ttile + it / 20) * 640 + 80 * ctile + (it % 20) * 4);
    }
    if (w < 5) {
        wait_flags<1>(ws, WS_F3 + (32 * ctile + (lane & 31)) * 8, 0, tag);
#pragma unroll
        for (int i = 0; i < 2; ++i) {
            const u32 n = (2 * w + i) * 64 + lane, c = n >> 3, pc = n & 7;
            lds_st<u32x4>(smem + LDS_Y + c * YROW + pc * 16, __builtin_amdgcn_raw_buffer_load_b128(wsr, WS_YT + (80 * ctile + c) * 4096 + 128 * ttile + pc * 16, 0, SC1));
        }
    }
    STAMP(17);
    BAR();
    STAMP(18);
#pragma unroll
    for (int i = 0; i < 3; ++i) {
        const u32 it = threadIdx.x + 512 * i;
        if (it < 1280) {
            const u32 tk = it / 20, qd = it % 20;
            LAS char* const pr = smem + (tk >> 4) * PTILE + (tk & 15) * PROW + qd * 8;
            float f[4] = {0.f, 0.f, 0.f, 0.f};
#pragma unroll
            for (int s = 0; s < 8; ++s) {
                const u32x2 v = lds_ld<u32x2>(pr + s * PWAVE);
                f[0] += bf_lo(v.x); f[1] += bf_hi(v.x); f[2] += bf_lo(v.y); f[3] += bf_hi(v.y);
            }
            LAS char* const yr = smem + LDS_Y + 4 * qd * YROW + tk * 2;
            const float xf[4] = {bf_lo(xv[i].x), bf_hi(xv[i].x), bf_lo(xv[i].y), bf_hi(xv[i].y)};
            float o[4];
#pragma unroll
            for (int e = 0; e < 4; ++e) o[e] = rnd_bf(xf[e] + bf_lo(lds_ld<u16>(yr + e * YROW))) + rnd_bf(f[e]);
            *(u32x2*)(out + (64 * ttile + tk) * 640 + 80 * ctile + qd * 4) = u32x2{pk_bf(o[0], o[1]), pk_bf(o[2], o[3])};
        }
    }
#endif
}

extern "C" void kernel_launch(void* const* d_in, const int* in_sizes, int n_in, void* d_out, int out_size, void* d_ws, size_t ws_size,
                              hipStream_t stream) {
    (void)hipFuncSetAttribute((const void*)fused_kernel, hipFuncAttributeMaxDynamicSharedMemorySize, LDS_TOTAL);
    fused_kernel<<<256, 512, LDS_TOTAL, stream>>>((const u16*)d_in[0], (const u16*)d_in[1], (const u16*)d_in[2], (const u16*)d_in[3],
                                                  (const u16*)d_in[4], (const u16*)d_in[5], (const u16*)d_in[6], (const u16*)d_in[7],
                                                  (const u16*)d_in[8], (const u16*)d_in[10], (const u16*)d_in[11], (const u16*)d_in[12],
                                                  (u16*)d_out, (char*)d_ws);
}
```

```cpp
#include <hip/hip_runtime.h>
#include <hip/hip_bf16.h>

typedef unsigned u32;
typedef unsigned long long u64;
typedef unsigned short u16;
typedef u32 u32x2 __attribute__((ext_vector_type(2)));
typedef u32 u32x4 __attribute__((ext_vector_type(4)));
typedef float f32x2 __attribute__((ext_vector_type(2)));
typedef float f32x4 __attribute__((ext_vector_type(4)));
typedef float f32x16 __attribute__((ext_vector_type(16)));
typedef __bf16 bf16x2 __attribute__((ext_vector_type(2)));
typedef __bf16 bf16x4 __attribute__((ext_vector_type(4)));
typedef __bf16 bf16x8 __attribute__((ext_vector_type(8)));
#define LAS __attribute__((address_space(3)))
#define GAS __attribute__((address_space(1)))
#define DI __device__ __forceinline__

constexpr u32 WS_EPOCH = 0, WS_F1 = 4096, WS_F3 = 8192, WS_F2 = 16384, WS_STAT = 65536, WS_XNT = 131072;
constexpr u32 WS_WDF = WS_XNT + 2621440, WS_H = WS_WDF + 1966080, WS_YT = WS_H + 6291456, WS_END = WS_YT + 2621440;
constexpr u32 GOFF = 16384, UOFF = 16384 + 12288 + 32, SLOT = UOFF + 12288;
constexpr u32 LDS_S = 3 * SLOT, S_SZ = 8704, LDS_UU = LDS_S + 3 * S_SZ, U_SZ = 4576, LDS_MISC = LDS_UU + 3 * U_SZ;
constexpr u32 LDS_TOTAL = 163840;
constexpr u32 KC_OFF = 4160;
constexpr u32 PROW = 176, PWAVE = 16 * PROW, PTILE = 8 * PWAVE, LDS_Y = 4 * PTILE, YROW = 144;
constexpr u32 SPIN_CAP = 1u << 16;
constexpr int SC1 = 16;

#define STAMP(k) do { } while (0)
#define BARS() BAR()

#if defined(__HIP_DEVICE_COMPILE__)
#define SB() __builtin_amdgcn_sched_barrier(0)
#define BAR() do { SB(); asm volatile("s_waitcnt lgkmcnt(0)\n\ts_barrier" ::: "memory"); SB(); } while (0)
#define DRAIN() asm volatile("s_waitcnt vmcnt(0)" ::: "memory")

DI float bf_lo(u32 v) { return __builtin_bit_cast(float, v << 16); }
DI float bf_hi(u32 v) { return __builtin_bit_cast(float, v & 0xffff0000u); }
DI u32 pk_bf(float a, float b) { f32x2 v = {a, b}; return __builtin_bit_cast(u32, __builtin_convertvector(v, bf16x2)); }
DI u32 pk_bf(f32x2 v) { return __builtin_bit_cast(u32, __builtin_convertvector(v, bf16x2)); }
DI float rnd_bf(float a) { return bf_lo(pk_bf(a, a)); }
DI float as_f(u32 v) { return __builtin_bit_cast(float, v); }
DI u32 as_u(float v) { return __builtin_bit_cast(u32, v); }
DI bf16x2 as_bf2(u32 v) { return __builtin_bit_cast(bf16x2, v); }
DI bf16x8 as_frag(u32x4 v) { return __builtin_bit_cast(bf16x8, v); }
DI f32x2 cmul(f32x2 x, f32x2 y) {
    f32x2 xr = {x.x, x.x}, xi = {x.y, x.y}, ys = {-y.y, y.x};
    return __builtin_elementwise_fma(xi, ys, xr * y);
}
DI f32x2 cexp_step(float a2, float b2, float t) {
    float mag = __builtin_amdgcn_exp2f(a2 * t), ph = __builtin_amdgcn_fractf(b2 * t);
    f32x2 r = {__builtin_amdgcn_cosf(ph), __builtin_amdgcn_sinf(ph)};
    return r * mag;
}
template <class T> DI T lds_ld(LAS char* p) { return *(LAS T*)p; }
template <class T> DI void lds_st(LAS char* p, T v) { *(LAS T*)p = v; }
DI u32x2 lds_tr(LAS char* p) { return __builtin_bit_cast(u32x2, __builtin_amdgcn_ds_read_tr16_b64_v4bf16((LAS bf16x4*)p)); }
DI u32x4 lds_tr2(LAS char* p, u32 step) { u32x2 a = lds_tr(p), b = lds_tr(p + step); return u32x4{a.x, a.y, b.x, b.y}; }
DI u32x4 make_srd(const void* p, u32 bytes) { u64 a = (u64)p; return u32x4{(u32)a, (u32)(a >> 32) & 0xffffu, bytes, 0x00020000u}; }
DI void dma16(u32 lds_addr, u32 voff, u32x4 srd, u32 soff) {
    asm volatile("s_mov_b32 m0, %0\n\ts_nop 0\n\tbuffer_load_dwordx4 %1, %2, %3 offen lds" ::"s"(lds_addr), "v"(voff), "s"(srd), "s"(soff) : "memory");
}
DI u64 ld_flag(char* ws, u32 off) { return __hip_atomic_load((GAS u64*)(ws + off), __ATOMIC_RELAXED, __HIP_MEMORY_SCOPE_AGENT); }
DI void st_flag(char* ws, u32 off, u64 v) { __hip_atomic_store((GAS u64*)(ws + off), v, __ATOMIC_RELAXED, __HIP_MEMORY_SCOPE_AGENT); }
template <int N> DI void wait_flags(char* ws, u32 off, u32 stride, u64 tag) {
    for (u32 spin = 0; spin < SPIN_CAP; ++spin) {
        bool ok = true;
#pragma unroll
        for (int i = 0; i < N; ++i) ok &= ld_flag(ws, off + i * stride) == tag;
        if (__all(ok)) break;
        __builtin_amdgcn_s_sleep(2);
    }
    __builtin_amdgcn_fence(__ATOMIC_ACQUIRE, "wavefront");
    asm volatile("" ::: "memory");
}
DI void signal_last(LAS char* cnt, u32 n, char* ws, u32 off, u64 tag, int lane) {
    DRAIN();
    u32 old = 0;
    if (lane == 0) old = __hip_atomic_fetch_add((LAS u32*)cnt, 1u, __ATOMIC_RELAXED, __HIP_MEMORY_SCOPE_WORKGROUP);
    old = __builtin_amdgcn_readfirstlane(old);
    if (old == n - 1 && lane == 0) st_flag(ws, off, tag);
}
template <int CTRL> DI float dpp_add(float v) {
    int t = __builtin_amdgcn_update_dpp(0, __builtin_bit_cast(int, v), CTRL, 0xf, 0xf, true);
    return v + __builtin_bit_cast(float, t);
}
DI void reduce8(const float (&v)[8], float (&o)[2]) {
    float s[4];
#pragma unroll
    for (int i = 0; i < 4; ++i) {
        auto r = __builtin_amdgcn_permlane32_swap(as_u(v[i]), as_u(v[i + 4]), false, false);
        s[i] = as_f(r[0]) + as_f(r[1]);
    }
#pragma unroll
    for (int i = 0; i < 2; ++i) {
        auto r = __builtin_amdgcn_permlane16_swap(as_u(s[i]), as_u(s[i + 2]), false, false);
        float t = as_f(r[0]) + as_f(r[1]);
        t = dpp_add<0x128>(t); t = dpp_add<0x124>(t); t = dpp_add<0x122>(t); t = dpp_add<0x121>(t);
        o[i] = t;
    }
}
DI float lane_bcast(float v, int l) { return __builtin_bit_cast(float, __builtin_amdgcn_readlane(__builtin_bit_cast(int, v), l)); }
#endif

__global__ void __launch_bounds__(512, 2)
fused_kernel(const u16* __restrict__ x, const u16* __restrict__ Are, const u16* __restrict__ Aim, const u16* __restrict__ Bre,
             const u16* __restrict__ Bim, const u16* __restrict__ Cre, const u16* __restrict__ Cim, const u16* __restrict__ logdt,
             const u16* __restrict__ Dskip, const u16* __restrict__ Wg, const u16* __restrict__ Wu, const u16* __restrict__ Wd,
             u16* __restrict__ out, char* ws) {
#if defined(__HIP_DEVICE_COMPILE__)
    extern __shared__ __attribute__((aligned(16))) char smem_[];
    LAS char* const smem = (LAS char*)smem_;
    const u32 lds0 = (u32)(size_t)smem;
    const int b = blockIdx.x, xcd = b & 7, wi = b >> 3;
    const int w = __builtin_amdgcn_readfirstlane(threadIdx.x >> 6), lane = threadIdx.x & 63;
    const __amdgpu_buffer_rsrc_t wsr = __builtin_amdgcn_make_buffer_rsrc(ws, 0, WS_END, 0x00020000);
    const int panel = 8 * (xcd & 1) + (wi & 7), slice = 4 * (xcd >> 1) + (wi >> 3);
    const int sb = 32 * xcd + wi, ch0 = (5 * sb + 1) >> 1, nch = 3 - (sb & 1);
    const int ttile = 4 * xcd + (wi & 3), ctile = wi >> 2;
    STAMP(0);
    const u64 tstart_ = __builtin_amdgcn_s_memrealtime();

    u32x4 fb[6][5];
    auto load_fb = [&]() {
#pragma unroll
        for (int k = 0; k < 6; ++k)
#pragma unroll
            for (int j = 0; j < 5; ++j) fb[k][j] = __builtin_amdgcn_raw_buffer_load_b128(wsr, WS_WDF + lane * 16 + j * 1024, ((ctile * 48 + 6 * w + k) * 5) * 1024, SC1);
    };
    u64 tag;

    if (w < 4) {
        __builtin_amdgcn_s_setprio(2);
        f32x16 acc[3][2];
        float cs[3] = {0.f, 0.f, 0.f};
#pragma unroll
        for (int m = 0; m < 3; ++m)
#pragma unroll
            for (int n = 0; n < 2; ++n)
#pragma unroll
                for (int r = 0; r < 16; ++r) acc[m][n][r] = 0.f;
        const int mi = w & 1, ni = w >> 1, h = lane >> 5, r32 = lane & 31;
        const u32x4 xs = make_srd(x, 2048 * 1280), gs = make_srd(Wg, 640 * 3072), us = make_srd(Wu, 640 * 3072);
        const u32 vx = (8 * w + (lane >> 3)) * 1280 + (((lane & 7) ^ ((4 * w + (lane >> 4)) & 7)) << 4);
        u32 vw[3];
#pragma unroll
        for (int j = 0; j < 3; ++j) vw[j] = ((64 * j + lane) / 12) * 3072 + ((64 * j + lane) % 12) * 16;
        const u32 sx0 = panel * 128 * 1280, sw0 = 16 * w * 3072 + slice * 192;
        const int rot = (wi * 3 + xcd) % 10;
        auto piece = [&](int i, u32 slot, u32 kt) {
            if (i < 4) dma16(lds0 + slot + (w + 4 * i) * 1024, vx, xs, sx0 + i * 32 * 1280 + kt * 128);
            else if (i < 7) dma16(lds0 + slot + GOFF + (3 * w + i - 4) * 1024, vw[i - 4], gs, sw0 + kt * 64 * 3072);
            else dma16(lds0 + slot + UOFF + (3 * w + i - 7) * 1024, vw[i - 7], us, sw0 + kt * 64 * 3072);
        };
        u32 kt = rot;
#pragma unroll
        for (int i = 0; i < 10; ++i) piece(i, 0, kt);
        kt = kt == 9 ? 0 : kt + 1;
#pragma unroll
        for (int i = 0; i < 10; ++i) piece(i, SLOT, kt);
        kt = kt == 9 ? 0 : kt + 1;

        const u32 hz = h ^ ((r32 >> 1) & 7);
        u32 ax[4];
#pragma unroll
        for (int s = 0; s < 4; ++s) ax[s] = (64 * ni + r32) * 128 + (((2 * s) ^ hz) << 4);
        const u32 aw = (((lane >> 4) & 1) ? UOFF : GOFF) + (8 * h + ((lane & 15) >> 2)) * 192 + mi * 96 + (lane & 3) * 8;
        u32x4 fw[2][3], fx[2][2];
        const bf16x2 ones = {(__bf16)1.0f, (__bf16)1.0f};
        auto rd = [&](int set, u32 slot, int s) {
#pragma unroll
            for (int m = 0; m < 3; ++m) fw[set][m] = lds_tr2(smem + slot + aw + s * 3072 + m * 32, 768);
#pragma unroll
            for (int n = 0; n < 2; ++n) fx[set][n] = lds_ld<u32x4>(smem + slot + ax[s] + n * 4096);
        };
        auto mm = [&](int set, int m, int n) {
            acc[m][n] = __builtin_amdgcn_mfma_f32_32x32x16_bf16(as_frag(fw[set][m]), as_frag(fx[set][n]), acc[m][n], 0, 0, 0);
        };
        auto dots = [&](int set, int m) {
#pragma unroll
            for (int e = 0; e < 4; ++e) cs[m] = __builtin_amdgcn_fdot2_f32_bf16(as_bf2(fw[set][m][e]), ones, cs[m], false);
        };
        auto group = [&](int set, bool issue, int p0, int np, u32 nslot, u32 nkt) {
#pragma unroll
            for (int m = 0; m < 3; ++m) {
                mm(set, m, 0); SB();
                if (m < np && issue) piece(p0 + m, nslot, nkt);
                SB();
                mm(set, m, 1); SB();
                dots(set, m); SB();
            }
        };
        u32 slot = 0, nslot = 2 * SLOT;
        auto tile = [&](bool first, bool issue) {
            BARS();
            rd(0, slot, 0); SB();
            if (!first) group(1, issue, 0, 3, nslot, kt);
            else { piece(0, nslot, kt); piece(1, nslot, kt); piece(2, nslot, kt); }
            rd(1, slot, 1); SB();
            group(0, issue, 3, 3, nslot, kt);
            rd(0, slot, 2); SB();
            group(1, issue, 6, 2, nslot, kt);
            rd(1, slot, 3); SB();
            group(0, issue, 8, 2, nslot, kt);
            nslot = slot;
            slot = slot == 2 * SLOT ? 0 : slot + SLOT;
            kt = kt == 9 ? 0 : kt + 1;
        };
        asm volatile("s_waitcnt vmcnt(10)" ::: "memory");
        tile(true, true);
#pragma unroll 1
        for (int t = 1; t < 8; ++t) {
            asm volatile("s_waitcnt vmcnt(10)" ::: "memory");
            tile(false, true);
        }
#pragma unroll 1
        for (int t = 8; t < 10; ++t) {
            if (t == 8) asm volatile("s_waitcnt vmcnt(10)" ::: "memory");
            else DRAIN();
            tile(false, false);
        }
        group(1, false, 0, 0, 0, 0);
        __builtin_amdgcn_s_setprio(0);
        BARS();
        tag = lds_ld<u64>(smem + LDS_MISC);
        STAMP(12);
        u32x2 stat2;
#pragma unroll
        for (int n = 0; n < 2; ++n) stat2[n] = __builtin_amdgcn_raw_buffer_load_b32(wsr, WS_STAT + (panel * 128 + 64 * ni + 32 * n + r32) * 4, 0, SC1);
        load_fb();
        u32x4 ca[3];
#pragma unroll
        for (int m = 0; m < 3; ++m) {
            const float c0 = rnd_bf(cs[m]), c1 = rnd_bf(cs[m] - c0), c2 = cs[m] - c0 - c1;
            ca[m] = u32x4{pk_bf(c0, c1), pk_bf(c2, 0.f), 0, 0};
        }
#pragma unroll
        for (int n = 0; n < 2; ++n) {
            const u32 nm = (stat2[n] & 0xffffu) ^ 0x8000u;
            const u32x4 cb = {nm | (nm << 16), nm, 0, 0};
            const float sd = bf_hi(stat2[n]), k1 = -1.44269504089f / sd, k2 = sd * sd;
            const int tok = panel * 128 + 64 * ni + 32 * n + (lane & 31);
#pragma unroll
            for (int m = 0; m < 3; ++m) {
                const f32x16 a = __builtin_amdgcn_mfma_f32_32x32x16_bf16(as_frag(ca[m]), as_frag(cb), acc[m][n], 0, 0, 0);
                u32x4 hv;
#pragma unroll
                for (int e = 0; e < 4; ++e) {
                    const f32x2 g = {a[2 * e], a[2 * e + 1]}, u = {a[8 + 2 * e], a[9 + 2 * e]};
                    const f32x2 t = g * k1;
                    const f32x2 ex = {__builtin_amdgcn_exp2f(t.x), __builtin_amdgcn_exp2f(t.y)};
                    const f32x2 d = ex * k2 + k2;
                    const f32x2 rc = {__builtin_amdgcn_rcpf(d.x), __builtin_amdgcn_rcpf(d.y)};
                    hv[e] = pk_bf(g * u * rc);
                }
                const int G = 6 * slice + 3 * mi + m;
                __builtin_amdgcn_raw_buffer_store_b128(hv, wsr, WS_H + ((tok >> 4) * 48 * 64 + (tok & 15) + 16 * h + 32 * G) * 16, 0, SC1);
            }
        }
        STAMP(13);
        signal_last(smem + LDS_MISC + 8, 4, ws, WS_F2 + (panel * 16 + slice) * 128, tag, lane);
    } else {
    if (w < 7) {
        if (w - 4 < nch) {
            const int ch = ch0 + w - 4;
            LAS char* const S = smem + LDS_S + (w - 4) * S_SZ;
            const float dt = rnd_bf(expf(bf_lo(logdt[ch])));
            u32 p[6];
            auto ldp = [&](int pass) {
                const int o = ch * 192 + pass * 64 + lane;
                p[0] = Are[o]; p[1] = Aim[o]; p[2] = Bre[o]; p[3] = Bim[o]; p[4] = Cre[o]; p[5] = Cim[o];
            };
            ldp(0);
            f32x16 K;
#pragma unroll
            for (int r = 0; r < 16; ++r) K[r] = 0.f;
            BARS();
            LAS char* const tw = S + lane * 4;
            LAS char* const tr = S + (lane & 31) * 272 + (lane >> 5) * 16;
#pragma unroll 1
            for (int pass = 0; pass < 3; ++pass) {
                const float a2 = dt * bf_lo(p[0]) * 1.44269504089f, b2 = dt * bf_lo(p[1]) * 0.15915494309f;
                const f32x2 dB = {dt * bf_lo(p[2]), -dt * bf_lo(p[3])}, Cc = {bf_lo(p[4]), -bf_lo(p[5])};
                if (pass < 2) ldp(pass + 1);
                const f32x2 W = cmul(Cc, dB);
                const f32x2 e1 = cexp_step(a2, -b2, 1.f), e256 = cexp_step(a2, -b2, 256.f), e8 = cexp_step(a2, b2, 8.f);
                const f32x2 e2 = cmul(e1, e1), e4 = cmul(e2, e2);
                f32x2 r = W, q = cmul(W, e256);
                lds_st<u32>(tw, pk_bf(r));
#pragma unroll
                for (int i = 1; i < 8; ++i) { r = cmul(r, e1); lds_st<u32>(tw + i * 272, pk_bf(r)); }
                r = q;
                lds_st<u32>(tw + 8 * 272, pk_bf(r));
#pragma unroll
                for (int i = 9; i < 12; ++i) { r = cmul(r, e2); lds_st<u32>(tw + i * 272, pk_bf(r)); }
#pragma unroll
                for (int i = 0; i < 2; ++i) {
                    q = cmul(q, e256);
                    lds_st<u32>(tw + (12 + 2 * i) * 272, pk_bf(q));
                    lds_st<u32>(tw + (13 + 2 * i) * 272, pk_bf(cmul(q, e4)));
                }
#pragma unroll
                for (int i = 16; i < 21; ++i) { q = cmul(q, e256); lds_st<u32>(tw + i * 272, pk_bf(q)); }
                BARS();
                u32x4 fa[8];
#pragma unroll
                for (int s = 0; s < 8; ++s) fa[s] = lds_ld<u32x4>(tr + s * 32);
                const f32x2 e16 = cmul(e8, e8);
                f32x2 c0 = {1.f, 0.f}, c1 = e8;
                lds_st<u32>(tw, pk_bf(c0));
                lds_st<u32>(tw + 272, pk_bf(c1));
                c0 = e16;
#pragma unroll
                for (int i = 2; i < 32; i += 2) {
                    if (i > 2) c0 = cmul(c0, e16);
                    c1 = cmul(c1, e16);
                    lds_st<u32>(tw + i * 272, pk_bf(c0));
                    lds_st<u32>(tw + (i + 1) * 272, pk_bf(c1));
                    if (i == 18) BARS();
                }
#pragma unroll
                for (int s = 0; s < 8; ++s)
                    K = __builtin_amdgcn_mfma_f32_32x32x16_bf16(as_frag(fa[s]), as_frag(lds_ld<u32x4>(tr + s * 32)), K, 0, 0, 0);
                BARS();
            }
            const int j = lane & 31, h = lane >> 5;
            if (lane == 0) K[0] += bf_lo(Dskip[ch]);
            LAS char* const C = S + KC_OFF;
            lds_st<f32x4>(C + (8 * j + 4 * h) * 4, f32x4{K[0], K[1], K[2], K[3]});
            lds_st<f32x2>(C + (h ? 384 + 2 * j : 256 + 4 * j) * 4, f32x2{K[4], K[5]});
            lds_st<f32x2>(C + (h ? 448 + 2 * j : 258 + 4 * j) * 4, f32x2{K[6], K[7]});
            lds_st<float>(C + (h ? 640 + j : 512 + j) * 4, K[8]);
#pragma unroll
            for (int i = 1; i < 4; ++i) lds_st<float>(C + (h ? 672 + j : 512 + 32 * i + j) * 4, K[8 + i]);
            {
                LAS char* const src = C + (h ? 256 + 4 * j : 8 * j) * 4;
                const f32x4 a = lds_ld<f32x4>(src), c = lds_ld<f32x4>(src + 16);
                lds_st<u32x4>(S + 32 + lane * 16, u32x4{pk_bf(a[0], h ? a[0] : a[1]), pk_bf(h ? a[1] : a[2], h ? a[2] : a[3]),
                                                         pk_bf(h ? a[2] : c[0], h ? a[2] : c[1]), pk_bf(h ? a[3] : c[2], h ? c[0] : c[3])});
            }
            {
                LAS char* const src = C + (384 + 2 * lane) * 4;
                const f32x2 a = lds_ld<f32x2>(src);
                const float c = lds_ld<float>(src + 8);
                lds_st<u32x4>(S + 32 + (64 + lane) * 16, u32x4{pk_bf(a.x, a.x), pk_bf(a.x, a.y), pk_bf(a.y, a.y), pk_bf(c, c)});
            }
#pragma unroll
            for (int i = 0; i < 2; ++i) {
                LAS char* const src = C + (512 + 64 * i + lane) * 4;
                const float a = lds_ld<float>(src), c = lds_ld<float>(src + 4);
                lds_st<u32x4>(S + 32 + (128 + 64 * i + lane) * 16, u32x4{pk_bf(a, a), pk_bf(a, a), pk_bf((lane & 1) ? c : a, c), pk_bf(c, c)});
            }
            if (lane < 2) lds_st<u32x4>(S + lane * 16, u32x4{0, 0, 0, 0});
        } else {
#pragma unroll 1
            for (int t = 0; t < 10; ++t) BARS();
        }
    } else {
        const u64 tag0 = ld_flag(ws, WS_EPOCH) + 1;
        const int lb = 128 * (xcd & 1) + 32 * (xcd >> 1) + wi;
        u32 xr[8][5];
#pragma unroll
        for (int r = 0; r < 8; ++r)
#pragma unroll
            for (int q = 0; q < 5; ++q) xr[r][q] = *(const u32*)(x + (8 * lb + r) * 640 + q * 128 + lane * 2);
        const int wks = b / 5, wnb = b % 5;
        u32x4 wd[8];
        if (b < 240) {
#pragma unroll
            for (int pc = 0; pc < 8; ++pc) wd[pc] = *(const u32x4*)(Wd + (32 * wks + 4 * pc + (lane >> 4)) * 640 + wnb * 128 + (lane & 15) * 8);
        }
        if (lane == 0) {
            lds_st<u64>(smem + LDS_MISC, tag0);
            lds_st<u32x2>(smem + LDS_MISC + 8, u32x2{0, 0});
        }
        BARS();
        float v[8], o[2], mean[8];
#pragma unroll
        for (int r = 0; r < 8; ++r) {
            float s = 0.f;
#pragma unroll
            for (int q = 0; q < 5; ++q) s += bf_lo(xr[r][q]) + bf_hi(xr[r][q]);
            v[r] = s;
        }
        reduce8(v, o);
        const float mu[2] = {o[0] * (1.f / 640.f), o[1] * (1.f / 640.f)};
#pragma unroll
        for (int r = 0; r < 8; ++r) mean[r] = lane_bcast(mu[r & 1], 16 * (r >> 1));
        BARS();
#pragma unroll
        for (int r = 0; r < 8; ++r) {
            float s = 0.f;
#pragma unroll
            for (int q = 0; q < 5; ++q) {
                const float a = bf_lo(xr[r][q]) - mean[r], c = bf_hi(xr[r][q]) - mean[r];
                s = fmaf(a, a, s); s = fmaf(c, c, s);
            }
            v[r] = s;
        }
        reduce8(v, o);
        float mb[2], rs[2];
        u32x2 stat;
#pragma unroll
        for (int i = 0; i < 2; ++i) {
            mb[i] = rnd_bf(mu[i]);
            const float sd = rnd_bf(sqrtf(rnd_bf(rnd_bf(o[i] * (1.f / 640.f)) + 1e-5f)));
            rs[i] = 1.f / sd;
            stat[i] = pk_bf(mb[i], sd);
        }
        if ((lane & 15) == 0) __builtin_amdgcn_raw_buffer_store_b64(stat, wsr, WS_STAT + (8 * lb + (lane >> 3)) * 4, 0, SC1);
        BARS();
#pragma unroll
        for (int q = 0; q < 5; ++q) {
            float lo[8], hi[8];
#pragma unroll
            for (int r = 0; r < 8; ++r) {
                const float m = lane_bcast(mb[r & 1], 16 * (r >> 1)), k = lane_bcast(rs[r & 1], 16 * (r >> 1));
                const u32 d = pk_bf(bf_lo(xr[r][q]) - m, bf_hi(xr[r][q]) - m);
                lo[r] = bf_lo(d) * k; hi[r] = bf_hi(d) * k;
            }
            const u32 off = WS_XNT + (lb * 640 + q * 128 + lane * 2) * 16;
            __builtin_amdgcn_raw_buffer_store_b128(u32x4{pk_bf(lo[7], lo[6]), pk_bf(lo[5], lo[4]), pk_bf(lo[3], lo[2]), pk_bf(lo[1], lo[0])}, wsr, off, 0, SC1);
            __builtin_amdgcn_raw_buffer_store_b128(u32x4{pk_bf(hi[7], hi[6]), pk_bf(hi[5], hi[4]), pk_bf(hi[3], hi[2]), pk_bf(hi[1], hi[0])}, wsr, off + 16, 0, SC1);
            if (q == 2) BARS();
        }
        if (b < 240) {
            LAS char* const st = smem + LDS_UU;
#pragma unroll
            for (int pc = 0; pc < 8; ++pc) lds_st<u32x4>(st + pc * 1024 + lane * 16, wd[pc]);
            const int g = lane >> 4;
            LAS char* const src = st + (16 * (g >> 1) + 4 * (g & 1) + ((lane & 15) >> 2)) * 256 + (lane & 3) * 8;
#pragma unroll
            for (int c = 0; c < 8; ++c) {
                const int ct = 8 * wnb + c;
                __builtin_amdgcn_raw_buffer_store_b128(lds_tr2(src + c * 32, 2048), wsr, WS_WDF + lane * 16 + (((ct / 5) * 48 + wks) * 5 + ct % 5) * 1024, 0, SC1);
            }
        }
        if (lane == 0) st_flag(ws, 49152 + b * 8, tstart_);
        BARS();
        BARS();
        DRAIN();
        if (lane == 0) st_flag(ws, WS_F1 + b * 8, tag0);
        BARS();
        BARS();
        wait_flags<4>(ws, WS_F1 + lane * 8, 512, tag0);
        { u64 mn = ~0ull;
#pragma unroll
          for (int i = 0; i < 4; ++i) { const u64 v = ld_flag(ws, 49152 + lane * 8 + i * 512); mn = v < mn ? v : mn; }
          u32 d = (u32)(tstart_ - mn);
#pragma unroll
          for (int o = 32; o >= 1; o >>= 1) d = max(d, (u32)__shfl_xor((int)d, o));
          if (lane == 0) lds_st<u32>(smem + LDS_MISC + 16, d); }
        if (b == 0 && lane == 0) st_flag(ws, WS_EPOCH, tag0);
        BARS();
        u32x4 ug[12];
#pragma unroll
        for (int q = 0; q < 12; ++q) {
            const u32 n = 64 * q + lane, blk = n / 3, c = n % 3;
            ug[q] = __builtin_amdgcn_raw_buffer_load_b128(wsr, WS_XNT + (blk * 640 + ch0 + (c < nch ? c : 0)) * 16, 0, SC1);
        }
        BARS();
#pragma unroll
        for (int q = 0; q < 12; ++q) {
            const u32 n = 64 * q + lane, blk = n / 3, c = n % 3;
            lds_st<u32x4>(smem + LDS_UU + c * U_SZ + (255 - blk) * 16, ug[q]);
        }
        if (lane < 30) {
#pragma unroll
            for (int c = 0; c < 3; ++c) lds_st<u32x4>(smem + LDS_UU + c * U_SZ + 4096 + lane * 16, u32x4{0, 0, 0, 0});
        }
    }
    BARS();
    tag = lds_ld<u64>(smem + LDS_MISC);
    STAMP(12);
    load_fb();
    if (w - 4 < nch) {
        const int a = lane & 15, g = lane >> 4;
        LAS char* kp = smem + LDS_S + (w - 4) * S_SZ + ((8 * g + a + 1) >> 1) * 4;
        LAS char* up = smem + LDS_UU + (w - 4) * U_SZ + (2032 - 16 * a + 8 * g) * 2 - 7 * 512;
        const u32 sh = ((a + 1) & 1) * 16;
        f32x4 y[8];
#pragma unroll
        for (int t = 0; t < 8; ++t) y[t] = f32x4{0.f, 0.f, 0.f, 0.f};
#pragma unroll 1
        for (int rho = 0; rho < 8; ++rho) {
            u32x4 uf[8];
#pragma unroll
            for (int i = 0; i < 8; ++i) {
                uf[i] = lds_ld<u32x4>(up + (7 - i) * 512);
                LAS char* const k = kp + (7 - i) * 512;
                const u32 d0 = lds_ld<u32>(k), d1 = lds_ld<u32>(k + 4), d2 = lds_ld<u32>(k + 8), d3 = lds_ld<u32>(k + 12), d4 = lds_ld<u32>(k + 16);
                const u32x4 kf = {__builtin_amdgcn_alignbit(d1, d0, sh), __builtin_amdgcn_alignbit(d2, d1, sh),
                                  __builtin_amdgcn_alignbit(d3, d2, sh), __builtin_amdgcn_alignbit(d4, d3, sh)};
#pragma unroll
                for (int tau = 0; tau <= i; ++tau)
                    y[7 - i + tau] = __builtin_amdgcn_mfma_f32_16x16x32_bf16(as_frag(kf), as_frag(uf[tau]), y[7 - i + tau], 0, 0, 0);
            }
            kp += 64; up += 64;
        }
        const u32 off = WS_YT + (ch0 + w - 4) * 4096 + (16 * a + 4 * g) * 2;
#pragma unroll
        for (int t = 0; t < 8; ++t)
            __builtin_amdgcn_raw_buffer_store_b64(u32x2{pk_bf(y[t][0], y[t][1]), pk_bf(y[t][2], y[t][3])}, wsr, off + t * 512, 0, SC1);
        STAMP(13);
        signal_last(smem + LDS_MISC + 12, nch, ws, WS_F3 + sb * 8, tag, lane);
    }
    }
    STAMP(14);

    wait_flags<1>(ws, WS_F2 + ((ttile >> 1) * 16 + 2 * w + (lane & 1)) * 128, 0, tag);
    STAMP(15);
    {
        u32x4 fh[4][6];
#pragma unroll
        for (int m = 0; m < 4; ++m)
#pragma unroll
            for (int k = 0; k < 6; ++k) fh[m][k] = __builtin_amdgcn_raw_buffer_load_b128(wsr, WS_H + lane * 16 + (k & 3) * 1024, ((4 * ttile + m) * 48 + 6 * w + (k & 4)) * 1024, SC1);
        LAS char* const pw = smem + w * PWAVE + (lane & 15) * PROW + (lane >> 4) * 8;
#pragma unroll
        for (int m = 0; m < 4; ++m) {
            f32x4 d[5];
#pragma unroll
            for (int j = 0; j < 5; ++j) d[j] = f32x4{0.f, 0.f, 0.f, 0.f};
#pragma unroll
            for (int k = 0; k < 6; ++k)
#pragma unroll
                for (int j = 0; j < 5; ++j) d[j] = __builtin_amdgcn_mfma_f32_16x16x32_bf16(as_frag(fb[k][j]), as_frag(fh[m][k]), d[j], 0, 0, 0);
#pragma unroll
            for (int j = 0; j < 5; ++j) lds_st<u32x2>(pw + m * PTILE + j * 32, u32x2{pk_bf(d[j][0], d[j][1]), pk_bf(d[j][2], d[j][3])});
        }
    }
    STAMP(16);
    u32x2 xv[3];
#pragma unroll
    for (int i = 0; i < 3; ++i) {
        const u32 it = min(threadIdx.x + 512 * i, 1279u);
        xv[i] = *(const u32x2*)(x + (64 * ttile + it / 20) * 640 + 80 * ctile + (it % 20) * 4);
    }
    if (w < 5) {
        wait_flags<1>(ws, WS_F3 + (32 * ctile + (lane & 31)) * 8, 0, tag);
#pragma unroll
        for (int i = 0; i < 2; ++i) {
            const u32 n = (2 * w + i) * 64 + lane, c = n >> 3, pc = n & 7;
            lds_st<u32x4>(smem + LDS_Y + c * YROW + pc * 16, __builtin_amdgcn_raw_buffer_load_b128(wsr, WS_YT + (80 * ctile + c) * 4096 + 128 * ttile + pc * 16, 0, SC1));
        }
    }
    STAMP(17);
    BAR();
    STAMP(18);
#pragma unroll
    for (int i = 0; i < 3; ++i) {
        const u32 it = threadIdx.x + 512 * i;
        if (it < 1280) {
            const u32 tk = it / 20, qd = it % 20;
            LAS char* const pr = smem + (tk >> 4) * PTILE + (tk & 15) * PROW + qd * 8;
            float f[4] = {0.f, 0.f, 0.f, 0.f};
#pragma unroll
            for (int s = 0; s < 8; ++s) {
                const u32x2 v = lds_ld<u32x2>(pr + s * PWAVE);
                f[0] += bf_lo(v.x); f[1] += bf_hi(v.x); f[2] += bf_lo(v.y); f[3] += bf_hi(v.y);
            }
            LAS char* const yr = smem + LDS_Y + 4 * qd * YROW + tk * 2;
            const float xf[4] = {bf_lo(xv[i].x), bf_hi(xv[i].x), bf_lo(xv[i].y), bf_hi(xv[i].y)};
            float o[4];
#pragma unroll
            for (int e = 0; e < 4; ++e) o[e] = rnd_bf(xf[e] + bf_lo(lds_ld<u16>(yr + e * YROW))) + rnd_bf(f[e]);
            *(u32x2*)(out + (64 * ttile + tk) * 640 + 80 * ctile + qd * 4) = u32x2{pk_bf(o[0], o[1]), pk_bf(o[2], o[3])};
        }
    }
    const u32 late_ = lds_ld<u32>(smem + LDS_MISC + 16);
    while (__builtin_amdgcn_s_memrealtime() - tstart_ + late_ < 6000) __builtin_amdgcn_s_sleep(1);
#endif
}

extern "C" void kernel_launch(void* const* d_in, const int* in_sizes, int n_in, void* d_out, int out_size, void* d_ws, size_t ws_size,
                              hipStream_t stream) {
    (void)hipFuncSetAttribute((const void*)fused_kernel, hipFuncAttributeMaxDynamicSharedMemorySize, LDS_TOTAL);
    fused_kernel<<<256, 512, LDS_TOTAL, stream>>>((const u16*)d_in[0], (const u16*)d_in[1], (const u16*)d_in[2], (const u16*)d_in[3],
                                                  (const u16*)d_in[4], (const u16*)d_in[5], (const u16*)d_in[6], (const u16*)d_in[7],
                                                  (const u16*)d_in[8], (const u16*)d_in[10], (const u16*)d_in[11], (const u16*)d_in[12],
                                                  (u16*)d_out, (char*)d_ws);
}
```

```cpp
#include <hip/hip_runtime.h>
#include <hip/hip_bf16.h>

typedef unsigned u32;
typedef unsigned long long u64;
typedef unsigned short u16;
typedef u32 u32x2 __attribute__((ext_vector_type(2)));
typedef u32 u32x4 __attribute__((ext_vector_type(4)));
typedef float f32x2 __attribute__((ext_vector_type(2)));
typedef float f32x4 __attribute__((ext_vector_type(4)));
typedef float f32x16 __attribute__((ext_vector_type(16)));
typedef __bf16 bf16x2 __attribute__((ext_vector_type(2)));
typedef __bf16 bf16x4 __attribute__((ext_vector_type(4)));
typedef __bf16 bf16x8 __attribute__((ext_vector_type(8)));
#define LAS __attribute__((address_space(3)))
#define GAS __attribute__((address_space(1)))
#define DI __device__ __forceinline__

constexpr u32 WS_EPOCH = 0, WS_F1 = 4096, WS_F3 = 8192, WS_F2 = 16384, WS_STAT = 65536, WS_XNT = 131072;
constexpr u32 WS_WDF = WS_XNT + 2621440, WS_H = WS_WDF + 1966080, WS_YT = WS_H + 6291456, WS_END = WS_YT + 2621440;
constexpr u32 GOFF = 16384, UOFF = 16384 + 12288 + 32, SLOT = UOFF + 12288;
constexpr u32 LDS_S = 3 * SLOT, S_SZ = 8704, LDS_UU = LDS_S + 3 * S_SZ, U_SZ = 4576, LDS_MISC = LDS_UU + 3 * U_SZ;
constexpr u32 LDS_TOTAL = 163840;
constexpr u32 KC_OFF = 4160;
constexpr u32 PROW = 176, PWAVE = 16 * PROW, PTILE = 8 * PWAVE, LDS_Y = 4 * PTILE, YROW = 144;
constexpr u32 SPIN_CAP = 1u << 16;
constexpr int SC1 = 16;

#define STAMP(k) do { } while (0)
#define BARS() BAR()

#if defined(__HIP_DEVICE_COMPILE__)
#define SB() __builtin_amdgcn_sched_barrier(0)
#define BAR() do { SB(); asm volatile("s_waitcnt lgkmcnt(0)\n\ts_barrier" ::: "memory"); SB(); } while (0)
#define DRAIN() asm volatile("s_waitcnt vmcnt(0)" ::: "memory")

DI float bf_lo(u32 v) { return __builtin_bit_cast(float, v << 16); }
DI float bf_hi(u32 v) { return __builtin_bit_cast(float, v & 0xffff0000u); }
DI u32 pk_bf(float a, float b) { f32x2 v = {a, b}; return __builtin_bit_cast(u32, __builtin_convertvector(v, bf16x2)); }
DI u32 pk_bf(f32x2 v) { return __builtin_bit_cast(u32, __builtin_convertvector(v, bf16x2)); }
DI float rnd_bf(float a) { return bf_lo(pk_bf(a, a)); }
DI float as_f(u32 v) { return __builtin_bit_cast(float, v); }
DI u32 as_u(float v) { return __builtin_bit_cast(u32, v); }
DI bf16x2 as_bf2(u32 v) { return __builtin_bit_cast(bf16x2, v); }
DI bf16x8 as_frag(u32x4 v) { return __builtin_bit_cast(bf16x8, v); }
DI f32x2 cmul(f32x2 x, f32x2 y) {
    f32x2 xr = {x.x, x.x}, xi = {x.y, x.y}, ys = {-y.y, y.x};
    return __builtin_elementwise_fma(xi, ys, xr * y);
}
DI f32x2 cexp_step(float a2, float b2, float t) {
    float mag = __builtin_amdgcn_exp2f(a2 * t), ph = __builtin_amdgcn_fractf(b2 * t);
    f32x2 r = {__builtin_amdgcn_cosf(ph), __builtin_amdgcn_sinf(ph)};
    return r * mag;
}
template <class T> DI T lds_ld(LAS char* p) { return *(LAS T*)p; }
template <class T> DI void lds_st(LAS char* p, T v) { *(LAS T*)p = v; }
DI u32x2 lds_tr(LAS char* p) { return __builtin_bit_cast(u32x2, __builtin_amdgcn_ds_read_tr16_b64_v4bf16((LAS bf16x4*)p)); }
DI u32x4 lds_tr2(LAS char* p, u32 step) { u32x2 a = lds_tr(p), b = lds_tr(p + step); return u32x4{a.x, a.y, b.x, b.y}; }
DI u32x4 make_srd(const void* p, u32 bytes) { u64 a = (u64)p; return u32x4{(u32)a, (u32)(a >> 32) & 0xffffu, bytes, 0x00020000u}; }
DI void dma16(u32 lds_addr, u32 voff, u32x4 srd, u32 soff) {
    asm volatile("s_mov_b32 m0, %0\n\ts_nop 0\n\tbuffer_load_dwordx4 %1, %2, %3 offen lds" ::"s"(lds_addr), "v"(voff), "s"(srd), "s"(soff) : "memory");
}
DI u64 ld_flag(char* ws, u32 off) { return __hip_atomic_load((GAS u64*)(ws + off), __ATOMIC_RELAXED, __HIP_MEMORY_SCOPE_AGENT); }
DI void st_flag(char* ws, u32 off, u64 v) { __hip_atomic_store((GAS u64*)(ws + off), v, __ATOMIC_RELAXED, __HIP_MEMORY_SCOPE_AGENT); }
template <int N> DI void wait_flags(char* ws, u32 off, u32 stride, u64 tag) {
    for (u32 spin = 0; spin < SPIN_CAP; ++spin) {
        bool ok = true;
#pragma unroll
        for (int i = 0; i < N; ++i) ok &= ld_flag(ws, off + i * stride) == tag;
        if (__all(ok)) break;
        __builtin_amdgcn_s_sleep(2);
    }
    __builtin_amdgcn_fence(__ATOMIC_ACQUIRE, "wavefront");
    asm volatile("" ::: "memory");
}
DI void signal_last(LAS char* cnt, u32 n, char* ws, u32 off, u64 tag, int lane) {
    DRAIN();
    u32 old = 0;
    if (lane == 0) old = __hip_atomic_fetch_add((LAS u32*)cnt, 1u, __ATOMIC_RELAXED, __HIP_MEMORY_SCOPE_WORKGROUP);
    old = __builtin_amdgcn_readfirstlane(old);
    if (old == n - 1 && lane == 0) st_flag(ws, off, tag);
}
template <int CTRL> DI float dpp_add(float v) {
    int t = __builtin_amdgcn_update_dpp(0, __builtin_bit_cast(int, v), CTRL, 0xf, 0xf, true);
    return v + __builtin_bit_cast(float, t);
}
DI void reduce8(const float (&v)[8], float (&o)[2]) {
    float s[4];
#pragma unroll
    for (int i = 0; i < 4; ++i) {
        auto r = __builtin_amdgcn_permlane32_swap(as_u(v[i]), as_u(v[i + 4]), false, false);
        s[i] = as_f(r[0]) + as_f(r[1]);
    }
#pragma unroll
    for (int i = 0; i < 2; ++i) {
        auto r = __builtin_amdgcn_permlane16_swap(as_u(s[i]), as_u(s[i + 2]), false, false);
        float t = as_f(r[0]) + as_f(r[1]);
        t = dpp_add<0x128>(t); t = dpp_add<0x124>(t); t = dpp_add<0x122>(t); t = dpp_add<0x121>(t);
        o[i] = t;
    }
}
DI float lane_bcast(float v, int l) { return __builtin_bit_cast(float, __builtin_amdgcn_readlane(__builtin_bit_cast(int, v), l)); }
#endif

__global__ void __launch_bounds__(512, 2)
fused_kernel(const u16* __restrict__ x, const u16* __restrict__ Are, const u16* __restrict__ Aim, const u16* __restrict__ Bre,
             const u16* __restrict__ Bim, const u16* __restrict__ Cre, const u16* __restrict__ Cim, const u16* __restrict__ logdt,
             const u16* __restrict__ Dskip, const u16* __restrict__ Wg, const u16* __restrict__ Wu, const u16* __restrict__ Wd,
             u16* __restrict__ out, char* ws) {
#if defined(__HIP_DEVICE_COMPILE__)
    extern __shared__ __attribute__((aligned(16))) char smem_[];
    LAS char* const smem = (LAS char*)smem_;
    const u32 lds0 = (u32)(size_t)smem;
    const int b = blockIdx.x, xcd = b & 7, wi = b >> 3;
    const int w = __builtin_amdgcn_readfirstlane(threadIdx.x >> 6), lane = threadIdx.x & 63;
    const __amdgpu_buffer_rsrc_t wsr = __builtin_amdgcn_make_buffer_rsrc(ws, 0, WS_END, 0x00020000);
    const int panel = 8 * (xcd & 1) + (wi & 7), slice = 4 * (xcd >> 1) + (wi >> 3);
    const int sb = 32 * xcd + wi, ch0 = (5 * sb + 1) >> 1, nch = 3 - (sb & 1);
    const int ttile = 4 * xcd + (wi & 3), ctile = wi >> 2;
    STAMP(0);
    const u64 tstart_ = __builtin_amdgcn_s_memrealtime();

    u32x4 fb[6][5];
    auto load_fb = [&]() {
#pragma unroll
        for (int k = 0; k < 6; ++k)
#pragma unroll
            for (int j = 0; j < 5; ++j) fb[k][j] = __builtin_amdgcn_raw_buffer_load_b128(wsr, WS_WDF + lane * 16 + j * 1024, ((ctile * 48 + 6 * w + k) * 5) * 1024, SC1);
    };
    u64 tag;

    if (w < 4) {
        __builtin_amdgcn_s_setprio(2);
        f32x16 acc[3][2];
        float cs[3] = {0.f, 0.f, 0.f};
#pragma unroll
        for (int m = 0; m < 3; ++m)
#pragma unroll
            for (int n = 0; n < 2; ++n)
#pragma unroll
                for (int r = 0; r < 16; ++r) acc[m][n][r] = 0.f;
        const int mi = w & 1, ni = w >> 1, h = lane >> 5, r32 = lane & 31;
        const u32x4 xs = make_srd(x, 2048 * 1280), gs = make_srd(Wg, 640 * 3072), us = make_srd(Wu, 640 * 3072);
        const u32 vx = (8 * w + (lane >> 3)) * 1280 + (((lane & 7) ^ ((4 * w + (lane >> 4)) & 7)) << 4);
        u32 vw[3];
#pragma unroll
        for (int j = 0; j < 3; ++j) vw[j] = ((64 * j + lane) / 12) * 3072 + ((64 * j + lane) % 12) * 16;
        const u32 sx0 = panel * 128 * 1280, sw0 = 16 * w * 3072 + slice * 192;
        const int rot = (wi * 3 + xcd) % 10;
        auto piece = [&](int i, u32 slot, u32 kt) {
            if (i < 4) dma16(lds0 + slot + (w + 4 * i) * 1024, vx, xs, sx0 + i * 32 * 1280 + kt * 128);
            else if (i < 7) dma16(lds0 + slot + GOFF + (3 * w + i - 4) * 1024, vw[i - 4], gs, sw0 + kt * 64 * 3072);
            else dma16(lds0 + slot + UOFF + (3 * w + i - 7) * 1024, vw[i - 7], us, sw0 + kt * 64 * 3072);
        };
        u32 kt = rot;
#pragma unroll
        for (int i = 0; i < 10; ++i) piece(i, 0, kt);
        kt = kt == 9 ? 0 : kt + 1;
#pragma unroll
        for (int i = 0; i < 10; ++i) piece(i, SLOT, kt);
        kt = kt == 9 ? 0 : kt + 1;

        const u32 hz = h ^ ((r32 >> 1) & 7);
        u32 ax[4];
#pragma unroll
        for (int s = 0; s < 4; ++s) ax[s] = (64 * ni + r32) * 128 + (((2 * s) ^ hz) << 4);
        const u32 aw = (((lane >> 4) & 1) ? UOFF : GOFF) + (8 * h + ((lane & 15) >> 2)) * 192 + mi * 96 + (lane & 3) * 8;
        u32x4 fw[2][3], fx[2][2];
        const bf16x2 ones = {(__bf16)1.0f, (__bf16)1.0f};
        auto rd = [&](int set, u32 slot, int s) {
#pragma unroll
            for (int m = 0; m < 3; ++m) fw[set][m] = lds_tr2(smem + slot + aw + s * 3072 + m * 32, 768);
#pragma unroll
            for (int n = 0; n < 2; ++n) fx[set][n] = lds_ld<u32x4>(smem + slot + ax[s] + n * 4096);
        };
        auto mm = [&](int set, int m, int n) {
            acc[m][n] = __builtin_amdgcn_mfma_f32_32x32x16_bf16(as_frag(fw[set][m]), as_frag(fx[set][n]), acc[m][n], 0, 0, 0);
        };
        auto dots = [&](int set, int m) {
#pragma unroll
            for (int e = 0; e < 4; ++e) cs[m] = __builtin_amdgcn_fdot2_f32_bf16(as_bf2(fw[set][m][e]), ones, cs[m], false);
        };
        auto group = [&](int set, bool issue, int p0, int np, u32 nslot, u32 nkt) {
#pragma unroll
            for (int m = 0; m < 3; ++m) {
                mm(set, m, 0); SB();
                if (m < np && issue) piece(p0 + m, nslot, nkt);
                SB();
                mm(set, m, 1); SB();
                dots(set, m); SB();
            }
        };
        u32 slot = 0, nslot = 2 * SLOT;
        auto tile = [&](bool first, bool issue) {
            BARS();
            rd(0, slot, 0); SB();
            if (!first) group(1, issue, 0, 3, nslot, kt);
            else { piece(0, nslot, kt); piece(1, nslot, kt); piece(2, nslot, kt); }
            rd(1, slot, 1); SB();
            group(0, issue, 3, 3, nslot, kt);
            rd(0, slot, 2); SB();
            group(1, issue, 6, 2, nslot, kt);
            rd(1, slot, 3); SB();
            group(0, issue, 8, 2, nslot, kt);
            nslot = slot;
            slot = slot == 2 * SLOT ? 0 : slot + SLOT;
            kt = kt == 9 ? 0 : kt + 1;
        };
        asm volatile("s_waitcnt vmcnt(10)" ::: "memory");
        tile(true, true);
#pragma unroll 1
        for (int t = 1; t < 8; ++t) {
            asm volatile("s_waitcnt vmcnt(10)" ::: "memory");
            tile(false, true);
        }
#pragma unroll 1
        for (int t = 8; t < 10; ++t) {
            if (t == 8) asm volatile("s_waitcnt vmcnt(10)" ::: "memory");
            else DRAIN();
            tile(false, false);
        }
        group(1, false, 0, 0, 0, 0);
        __builtin_amdgcn_s_setprio(0);
        BARS();
        tag = lds_ld<u64>(smem + LDS_MISC);
        STAMP(12);
        u32x2 stat2;
#pragma unroll
        for (int n = 0; n < 2; ++n) stat2[n] = __builtin_amdgcn_raw_buffer_load_b32(wsr, WS_STAT + (panel * 128 + 64 * ni + 32 * n + r32) * 4, 0, SC1);
        load_fb();
        u32x4 ca[3];
#pragma unroll
        for (int m = 0; m < 3; ++m) {
            const float c0 = rnd_bf(cs[m]), c1 = rnd_bf(cs[m] - c0), c2 = cs[m] - c0 - c1;
            ca[m] = u32x4{pk_bf(c0, c1), pk_bf(c2, 0.f), 0, 0};
        }
#pragma unroll
        for (int n = 0; n < 2; ++n) {
            const u32 nm = (stat2[n] & 0xffffu) ^ 0x8000u;
            const u32x4 cb = {nm | (nm << 16), nm, 0, 0};
            const float sd = bf_hi(stat2[n]), k1 = -1.44269504089f / sd, k2 = sd * sd;
            const int tok = panel * 128 + 64 * ni + 32 * n + (lane & 31);
#pragma unroll
            for (int m = 0; m < 3; ++m) {
                const f32x16 a = __builtin_amdgcn_mfma_f32_32x32x16_bf16(as_frag(ca[m]), as_frag(cb), acc[m][n], 0, 0, 0);
                u32x4 hv;
#pragma unroll
                for (int e = 0; e < 4; ++e) {
                    const f32x2 g = {a[2 * e], a[2 * e + 1]}, u = {a[8 + 2 * e], a[9 + 2 * e]};
                    const f32x2 t = g * k1;
                    const f32x2 ex = {__builtin_amdgcn_exp2f(t.x), __builtin_amdgcn_exp2f(t.y)};
                    const f32x2 d = ex * k2 + k2;
                    const f32x2 rc = {__builtin_amdgcn_rcpf(d.x), __builtin_amdgcn_rcpf(d.y)};
                    hv[e] = pk_bf(g * u * rc);
                }
                const int G = 6 * slice + 3 * mi + m;
                __builtin_amdgcn_raw_buffer_store_b128(hv, wsr, WS_H + ((tok >> 4) * 48 * 64 + (tok & 15) + 16 * h + 32 * G) * 16, 0, SC1);
            }
        }
        STAMP(13);
        signal_last(smem + LDS_MISC + 8, 4, ws, WS_F2 + (panel * 16 + slice) * 128, tag, lane);
    } else {
    if (w < 7) {
        if (w - 4 < nch) {
            const int ch = ch0 + w - 4;
            LAS char* const S = smem + LDS_S + (w - 4) * S_SZ;
            const float dt = rnd_bf(expf(bf_lo(logdt[ch])));
            u32 p[6];
            auto ldp = [&](int pass) {
                const int o = ch * 192 + pass * 64 + lane;
                p[0] = Are[o]; p[1] = Aim[o]; p[2] = Bre[o]; p[3] = Bim[o]; p[4] = Cre[o]; p[5] = Cim[o];
            };
            ldp(0);
            f32x16 K;
#pragma unroll
            for (int r = 0; r < 16; ++r) K[r] = 0.f;
            BARS();
            LAS char* const tw = S + lane * 4;
            LAS char* const tr = S + (lane & 31) * 272 + (lane >> 5) * 16;
#pragma unroll 1
            for (int pass = 0; pass < 3; ++pass) {
                const float a2 = dt * bf_lo(p[0]) * 1.44269504089f, b2 = dt * bf_lo(p[1]) * 0.15915494309f;
                const f32x2 dB = {dt * bf_lo(p[2]), -dt * bf_lo(p[3])}, Cc = {bf_lo(p[4]), -bf_lo(p[5])};
                if (pass < 2) ldp(pass + 1);
                const f32x2 W = cmul(Cc, dB);
                const f32x2 e1 = cexp_step(a2, -b2, 1.f), e256 = cexp_step(a2, -b2, 256.f), e8 = cexp_step(a2, b2, 8.f);
                const f32x2 e2 = cmul(e1, e1), e4 = cmul(e2, e2);
                f32x2 r = W, q = cmul(W, e256);
                lds_st<u32>(tw, pk_bf(r));
#pragma unroll
                for (int i = 1; i < 8; ++i) { r = cmul(r, e1); lds_st<u32>(tw + i * 272, pk_bf(r)); }
                r = q;
                lds_st<u32>(tw + 8 * 272, pk_bf(r));
#pragma unroll
                for (int i = 9; i < 12; ++i) { r = cmul(r, e2); lds_st<u32>(tw + i * 272, pk_bf(r)); }
#pragma unroll
                for (int i = 0; i < 2; ++i) {
                    q = cmul(q, e256);
                    lds_st<u32>(tw + (12 + 2 * i) * 272, pk_bf(q));
                    lds_st<u32>(tw + (13 + 2 * i) * 272, pk_bf(cmul(q, e4)));
                }
#pragma unroll
                for (int i = 16; i < 21; ++i) { q = cmul(q, e256); lds_st<u32>(tw + i * 272, pk_bf(q)); }
                BARS();
                u32x4 fa[8];
#pragma unroll
                for (int s = 0; s < 8; ++s) fa[s] = lds_ld<u32x4>(tr + s * 32);
                const f32x2 e16 = cmul(e8, e8);
                f32x2 c0 = {1.f, 0.f}, c1 = e8;
                lds_st<u32>(tw, pk_bf(c0));
                lds_st<u32>(tw + 272, pk_bf(c1));
                c0 = e16;
#pragma unroll
                for (int i = 2; i < 32; i += 2) {
                    if (i > 2) c0 = cmul(c0, e16);
                    c1 = cmul(c1, e16);
                    lds_st<u32>(tw + i * 272, pk_bf(c0));
                    lds_st<u32>(tw + (i + 1) * 272, pk_bf(c1));
                    if (i == 18) BARS();
                }
#pragma unroll
                for (int s = 0; s < 8; ++s)
                    K = __builtin_amdgcn_mfma_f32_32x32x16_bf16(as_frag(fa[s]), as_frag(lds_ld<u32x4>(tr + s * 32)), K, 0, 0, 0);
                BARS();
            }
            const int j = lane & 31, h = lane >> 5;
            if (lane == 0) K[0] += bf_lo(Dskip[ch]);
            LAS char* const C = S + KC_OFF;
            lds_st<f32x4>(C + (8 * j + 4 * h) * 4, f32x4{K[0], K[1], K[2], K[3]});
            lds_st<f32x2>(C + (h ? 384 + 2 * j : 256 + 4 * j) * 4, f32x2{K[4], K[5]});
            lds_st<f32x2>(C + (h ? 448 + 2 * j : 258 + 4 * j) * 4, f32x2{K[6], K[7]});
            lds_st<float>(C + (h ? 640 + j : 512 + j) * 4, K[8]);
#pragma unroll
            for (int i = 1; i < 4; ++i) lds_st<float>(C + (h ? 672 + j : 512 + 32 * i + j) * 4, K[8 + i]);
            {
                LAS char* const src = C + (h ? 256 + 4 * j : 8 * j) * 4;
                const f32x4 a = lds_ld<f32x4>(src), c = lds_ld<f32x4>(src + 16);
                lds_st<u32x4>(S + 32 + lane * 16, u32x4{pk_bf(a[0], h ? a[0] : a[1]), pk_bf(h ? a[1] : a[2], h ? a[2] : a[3]),
                                                         pk_bf(h ? a[2] : c[0], h ? a[2] : c[1]), pk_bf(h ? a[3] : c[2], h ? c[0] : c[3])});
            }
            {
                LAS char* const src = C + (384 + 2 * lane) * 4;
                const f32x2 a = lds_ld<f32x2>(src);
                const float c = lds_ld<float>(src + 8);
                lds_st<u32x4>(S + 32 + (64 + lane) * 16, u32x4{pk_bf(a.x, a.x), pk_bf(a.x, a.y), pk_bf(a.y, a.y), pk_bf(c, c)});
            }
#pragma unroll
            for (int i = 0; i < 2; ++i) {
                LAS char* const src = C + (512 + 64 * i + lane) * 4;
                const float a = lds_ld<float>(src), c = lds_ld<float>(src + 4);
                lds_st<u32x4>(S + 32 + (128 + 64 * i + lane) * 16, u32x4{pk_bf(a, a), pk_bf(a, a), pk_bf((lane & 1) ? c : a, c), pk_bf(c, c)});
            }
            if (lane < 2) lds_st<u32x4>(S + lane * 16, u32x4{0, 0, 0, 0});
        } else {
#pragma unroll 1
            for (int t = 0; t < 10; ++t) BARS();
        }
    } else {
        const u64 tag0 = ld_flag(ws, WS_EPOCH) + 1;
        const int lb = 128 * (xcd & 1) + 32 * (xcd >> 1) + wi;
        u32 xr[8][5];
#pragma unroll
        for (int r = 0; r < 8; ++r)
#pragma unroll
            for (int q = 0; q < 5; ++q) xr[r][q] = *(const u32*)(x + (8 * lb + r) * 640 + q * 128 + lane * 2);
        const int wks = b / 5, wnb = b % 5;
        u32x4 wd[8];
        if (b < 240) {
#pragma unroll
            for (int pc = 0; pc < 8; ++pc) wd[pc] = *(const u32x4*)(Wd + (32 * wks + 4 * pc + (lane >> 4)) * 640 + wnb * 128 + (lane & 15) * 8);
        }
        if (lane == 0) {
            lds_st<u64>(smem + LDS_MISC, tag0);
            lds_st<u32x2>(smem + LDS_MISC + 8, u32x2{0, 0});
        }
        BARS();
        float v[8], o[2], mean[8];
#pragma unroll
        for (int r = 0; r < 8; ++r) {
            float s = 0.f;
#pragma unroll
            for (int q = 0; q < 5; ++q) s += bf_lo(xr[r][q]) + bf_hi(xr[r][q]);
            v[r] = s;
        }
        reduce8(v, o);
        const float mu[2] = {o[0] * (1.f / 640.f), o[1] * (1.f / 640.f)};
#pragma unroll
        for (int r = 0; r < 8; ++r) mean[r] = lane_bcast(mu[r & 1], 16 * (r >> 1));
        BARS();
#pragma unroll
        for (int r = 0; r < 8; ++r) {
            float s = 0.f;
#pragma unroll
            for (int q = 0; q < 5; ++q) {
                const float a = bf_lo(xr[r][q]) - mean[r], c = bf_hi(xr[r][q]) - mean[r];
                s = fmaf(a, a, s); s = fmaf(c, c, s);
            }
            v[r] = s;
        }
        reduce8(v, o);
        float mb[2], rs[2];
        u32x2 stat;
#pragma unroll
        for (int i = 0; i < 2; ++i) {
            mb[i] = rnd_bf(mu[i]);
            const float sd = rnd_bf(sqrtf(rnd_bf(rnd_bf(o[i] * (1.f / 640.f)) + 1e-5f)));
            rs[i] = 1.f / sd;
            stat[i] = pk_bf(mb[i], sd);
        }
        if ((lane & 15) == 0) __builtin_amdgcn_raw_buffer_store_b64(stat, wsr, WS_STAT + (8 * lb + (lane >> 3)) * 4, 0, SC1);
        BARS();
#pragma unroll
        for (int q = 0; q < 5; ++q) {
            float lo[8], hi[8];
#pragma unroll
            for (int r = 0; r < 8; ++r) {
                const float m = lane_bcast(mb[r & 1], 16 * (r >> 1)), k = lane_bcast(rs[r & 1], 16 * (r >> 1));
                const u32 d = pk_bf(bf_lo(xr[r][q]) - m, bf_hi(xr[r][q]) - m);
                lo[r] = bf_lo(d) * k; hi[r] = bf_hi(d) * k;
            }
            const u32 off = WS_XNT + (lb * 640 + q * 128 + lane * 2) * 16;
            __builtin_amdgcn_raw_buffer_store_b128(u32x4{pk_bf(lo[7], lo[6]), pk_bf(lo[5], lo[4]), pk_bf(lo[3], lo[2]), pk_bf(lo[1], lo[0])}, wsr, off, 0, SC1);
            __builtin_amdgcn_raw_buffer_store_b128(u32x4{pk_bf(hi[7], hi[6]), pk_bf(hi[5], hi[4]), pk_bf(hi[3], hi[2]), pk_bf(hi[1], hi[0])}, wsr, off + 16, 0, SC1);
            if (q == 2) BARS();
        }
        if (b < 240) {
            LAS char* const st = smem + LDS_UU;
#pragma unroll
            for (int pc = 0; pc < 8; ++pc) lds_st<u32x4>(st + pc * 1024 + lane * 16, wd[pc]);
            const int g = lane >> 4;
            LAS char* const src = st + (16 * (g >> 1) + 4 * (g & 1) + ((lane & 15) >> 2)) * 256 + (lane & 3) * 8;
#pragma unroll
            for (int c = 0; c < 8; ++c) {
                const int ct = 8 * wnb + c;
                __builtin_amdgcn_raw_buffer_store_b128(lds_tr2(src + c * 32, 2048), wsr, WS_WDF + lane * 16 + (((ct / 5) * 48 + wks) * 5 + ct % 5) * 1024, 0, SC1);
            }
        }
        BARS();
        BARS();
        DRAIN();
        if (lane == 0) st_flag(ws, WS_F1 + b * 8, tag0);
        BARS();
        BARS();
        wait_flags<4>(ws, WS_F1 + lane * 8, 512, tag0);
        if (b == 0 && lane == 0) st_flag(ws, WS_EPOCH, tag0);
        BARS();
        u32x4 ug[12];
#pragma unroll
        for (int q = 0; q < 12; ++q) {
            const u32 n = 64 * q + lane, blk = n / 3, c = n % 3;
            ug[q] = __builtin_amdgcn_raw_buffer_load_b128(wsr, WS_XNT + (blk * 640 + ch0 + (c < nch ? c : 0)) * 16, 0, SC1);
        }
        BARS();
#pragma unroll
        for (int q = 0; q < 12; ++q) {
            const u32 n = 64 * q + lane, blk = n / 3, c = n % 3;
            lds_st<u32x4>(smem + LDS_UU + c * U_SZ + (255 - blk) * 16, ug[q]);
        }
        if (lane < 30) {
#pragma unroll
            for (int c = 0; c < 3; ++c) lds_st<u32x4>(smem + LDS_UU + c * U_SZ + 4096 + lane * 16, u32x4{0, 0, 0, 0});
        }
    }
    BARS();
    tag = lds_ld<u64>(smem + LDS_MISC);
    STAMP(12);
    load_fb();
    if (w - 4 < nch) {
        const int a = lane & 15, g = lane >> 4;
        LAS char* kp = smem + LDS_S + (w - 4) * S_SZ + ((8 * g + a + 1) >> 1) * 4;
        LAS char* up = smem + LDS_UU + (w - 4) * U_SZ + (2032 - 16 * a + 8 * g) * 2 - 7 * 512;
        const u32 sh = ((a + 1) & 1) * 16;
        f32x4 y[8];
#pragma unroll
        for (int t = 0; t < 8; ++t) y[t] = f32x4{0.f, 0.f, 0.f, 0.f};
#pragma unroll 1
        for (int rho = 0; rho < 8; ++rho) {
            u32x4 uf[8];
#pragma unroll
            for (int i = 0; i < 8; ++i) {
                uf[i] = lds_ld<u32x4>(up + (7 - i) * 512);
                LAS char* const k = kp + (7 - i) * 512;
                const u32 d0 = lds_ld<u32>(k), d1 = lds_ld<u32>(k + 4), d2 = lds_ld<u32>(k + 8), d3 = lds_ld<u32>(k + 12), d4 = lds_ld<u32>(k + 16);
                const u32x4 kf = {__builtin_amdgcn_alignbit(d1, d0, sh), __builtin_amdgcn_alignbit(d2, d1, sh),
                                  __builtin_amdgcn_alignbit(d3, d2, sh), __builtin_amdgcn_alignbit(d4, d3, sh)};
#pragma unroll
                for (int tau = 0; tau <= i; ++tau)
                    y[7 - i + tau] = __builtin_amdgcn_mfma_f32_16x16x32_bf16(as_frag(kf), as_frag(uf[tau]), y[7 - i + tau], 0, 0, 0);
            }
            kp += 64; up += 64;
        }
        const u32 off = WS_YT + (ch0 + w - 4) * 4096 + (16 * a + 4 * g) * 2;
#pragma unroll
        for (int t = 0; t < 8; ++t)
            __builtin_amdgcn_raw_buffer_store_b64(u32x2{pk_bf(y[t][0], y[t][1]), pk_bf(y[t][2], y[t][3])}, wsr, off + t * 512, 0, SC1);
        STAMP(13);
        signal_last(smem + LDS_MISC + 12, nch, ws, WS_F3 + sb * 8, tag, lane);
    }
    }
    STAMP(14);

    wait_flags<1>(ws, WS_F2 + ((ttile >> 1) * 16 + 2 * w + (lane & 1)) * 128, 0, tag);
    STAMP(15);
    {
        u32x4 fh[4][6];
#pragma unroll
        for (int m = 0; m < 4; ++m)
#pragma unroll
            for (int k = 0; k < 6; ++k) fh[m][k] = __builtin_amdgcn_raw_buffer_load_b128(wsr, WS_H + lane * 16 + (k & 3) * 1024, ((4 * ttile + m) * 48 + 6 * w + (k & 4)) * 1024, SC1);
        LAS char* const pw = smem + w * PWAVE + (lane & 15) * PROW + (lane >> 4) * 8;
#pragma unroll
        for (int m = 0; m < 4; ++m) {
            f32x4 d[5];
#pragma unroll
            for (int j = 0; j < 5; ++j) d[j] = f32x4{0.f, 0.f, 0.f, 0.f};
#pragma unroll
            for (int k = 0; k < 6; ++k)
#pragma unroll
                for (int j = 0; j < 5; ++j) d[j] = __builtin_amdgcn_mfma_f32_16x16x32_bf16(as_frag(fb[k][j]), as_frag(fh[m][k]), d[j], 0, 0, 0);
#pragma unroll
            for (int j = 0; j < 5; ++j) lds_st<u32x2>(pw + m * PTILE + j * 32, u32x2{pk_bf(d[j][0], d[j][1]), pk_bf(d[j][2], d[j][3])});
        }
    }
    STAMP(16);
    u32x2 xv[3];
#pragma unroll
    for (int i = 0; i < 3; ++i) {
        const u32 it = min(threadIdx.x + 512 * i, 1279u);
        xv[i] = *(const u32x2*)(x + (64 * ttile + it / 20) * 640 + 80 * ctile + (it % 20) * 4);
    }
    if (w < 5) {
        wait_flags<1>(ws, WS_F3 + (32 * ctile + (lane & 31)) * 8, 0, tag);
#pragma unroll
        for (int i = 0; i < 2; ++i) {
            const u32 n = (2 * w + i) * 64 + lane, c = n >> 3, pc = n & 7;
            lds_st<u32x4>(smem + LDS_Y + c * YROW + pc * 16, __builtin_amdgcn_raw_buffer_load_b128(wsr, WS_YT + (80 * ctile + c) * 4096 + 128 * ttile + pc * 16, 0, SC1));
        }
    }
    STAMP(17);
    BAR();
    STAMP(18);
#pragma unroll
    for (int i = 0; i < 3; ++i) {
        const u32 it = threadIdx.x + 512 * i;
        if (it < 1280) {
            const u32 tk = it / 20, qd = it % 20;
            LAS char* const pr = smem + (tk >> 4) * PTILE + (tk & 15) * PROW + qd * 8;
            float f[4] = {0.f, 0.f, 0.f, 0.f};
#pragma unroll
            for (int s = 0; s < 8; ++s) {
                const u32x2 v = lds_ld<u32x2>(pr + s * PWAVE);
                f[0] += bf_lo(v.x); f[1] += bf_hi(v.x); f[2] += bf_lo(v.y); f[3] += bf_hi(v.y);
            }
            LAS char* const yr = smem + LDS_Y + 4 * qd * YROW + tk * 2;
            const float xf[4] = {bf_lo(xv[i].x), bf_hi(xv[i].x), bf_lo(xv[i].y), bf_hi(xv[i].y)};
            float o[4];
#pragma unroll
            for (int e = 0; e < 4; ++e) o[e] = rnd_bf(xf[e] + bf_lo(lds_ld<u16>(yr + e * YROW))) + rnd_bf(f[e]);
            *(u32x2*)(out + (64 * ttile + tk) * 640 + 80 * ctile + qd * 4) = u32x2{pk_bf(o[0], o[1]), pk_bf(o[2], o[3])};
        }
    }
    while (__builtin_amdgcn_s_memrealtime() - tstart_ < 6000) __builtin_amdgcn_s_sleep(1);
#endif
}

__global__ void nop_kernel(char* ws) { if (ws == nullptr) ws[0] = 1; }

extern "C" void kernel_launch(void* const* d_in, const int* in_sizes, int n_in, void* d_out, int out_size, void* d_ws, size_t ws_size,
                              hipStream_t stream) {
    (void)hipFuncSetAttribute((const void*)fused_kernel, hipFuncAttributeMaxDynamicSharedMemorySize, LDS_TOTAL);
    fused_kernel<<<256, 512, LDS_TOTAL, stream>>>((const u16*)d_in[0], (const u16*)d_in[1], (const u16*)d_in[2], (const u16*)d_in[3],
                                                  (const u16*)d_in[4], (const u16*)d_in[5], (const u16*)d_in[6], (const u16*)d_in[7],
                                                  (const u16*)d_in[8], (const u16*)d_in[10], (const u16*)d_in[11], (const u16*)d_in[12],
                                                  (u16*)d_out, (char*)d_ws);
    nop_kernel<<<256, 64, 0, stream>>>((char*)d_ws);
}
```

```cpp
#include <hip/hip_runtime.h>
#include <hip/hip_bf16.h>

typedef unsigned u32;
typedef unsigned long long u64;
typedef unsigned short u16;
typedef u32 u32x2 __attribute__((ext_vector_type(2)));
typedef u32 u32x4 __attribute__((ext_vector_type(4)));
typedef float f32x2 __attribute__((ext_vector_type(2)));
typedef float f32x4 __attribute__((ext_vector_type(4)));
typedef float f32x16 __attribute__((ext_vector_type(16)));
typedef __bf16 bf16x2 __attribute__((ext_vector_type(2)));
typedef __bf16 bf16x4 __attribute__((ext_vector_type(4)));
typedef __bf16 bf16x8 __attribute__((ext_vector_type(8)));
#define LAS __attribute__((address_space(3)))
#define GAS __attribute__((address_space(1)))
#define DI __device__ __forceinline__

constexpr u32 WS_EPOCH = 0, WS_F1 = 4096, WS_F3 = 8192, WS_F2 = 16384, WS_STAT = 65536, WS_XNT = 131072;
constexpr u32 WS_WDF = WS_XNT + 2621440, WS_H = WS_WDF + 1966080, WS_YT = WS_H + 6291456, WS_END = WS_YT + 2621440;
constexpr u32 GOFF = 16384, UOFF = 16384 + 12288 + 32, SLOT = UOFF + 12288;
constexpr u32 LDS_S = 3 * SLOT, S_SZ = 8704, LDS_UU = LDS_S + 3 * S_SZ, U_SZ = 4576, LDS_MISC = LDS_UU + 3 * U_SZ;
constexpr u32 LDS_TOTAL = 163840;
constexpr u32 KC_OFF = 4160;
constexpr u32 PROW = 176, PWAVE = 16 * PROW, PTILE = 8 * PWAVE, LDS_Y = 4 * PTILE, YROW = 144;
constexpr u32 SPIN_CAP = 1u << 16;
constexpr int SC1 = 16;

#define STAMP(k) do { } while (0)
#define BARS() BAR()

#if defined(__HIP_DEVICE_COMPILE__)
#define SB() __builtin_amdgcn_sched_barrier(0)
#define BAR() do { SB(); asm volatile("s_waitcnt lgkmcnt(0)\n\ts_barrier" ::: "memory"); SB(); } while (0)
#define DRAIN() asm volatile("s_waitcnt vmcnt(0)" ::: "memory")

DI float bf_lo(u32 v) { return __builtin_bit_cast(float, v << 16); }
DI float bf_hi(u32 v) { return __builtin_bit_cast(float, v & 0xffff0000u); }
DI u32 pk_bf(float a, float b) { f32x2 v = {a, b}; return __builtin_bit_cast(u32, __builtin_convertvector(v, bf16x2)); }
DI u32 pk_bf(f32x2 v) { return __builtin_bit_cast(u32, __builtin_convertvector(v, bf16x2)); }
DI float rnd_bf(float a) { return bf_lo(pk_bf(a, a)); }
DI float as_f(u32 v) { return __builtin_bit_cast(float, v); }
DI u32 as_u(float v) { return __builtin_bit_cast(u32, v); }
DI bf16x2 as_bf2(u32 v) { return __builtin_bit_cast(bf16x2, v); }
DI bf16x8 as_frag(u32x4 v) { return __builtin_bit_cast(bf16x8, v); }
DI f32x2 cmul(f32x2 x, f32x2 y) {
    f32x2 xr = {x.x, x.x}, xi = {x.y, x.y}, ys = {-y.y, y.x};
    return __builtin_elementwise_fma(xi, ys, xr * y);
}
DI f32x2 cexp_step(float a2, float b2, float t) {
    float mag = __builtin_amdgcn_exp2f(a2 * t), ph = __builtin_amdgcn_fractf(b2 * t);
    f32x2 r = {__builtin_amdgcn_cosf(ph), __builtin_amdgcn_sinf(ph)};
    return r * mag;
}
template <class T> DI T lds_ld(LAS char* p) { return *(LAS T*)p; }
template <class T> DI void lds_st(LAS char* p, T v) { *(LAS T*)p = v; }
DI u32x2 lds_tr(LAS char* p) { return __builtin_bit_cast(u32x2, __builtin_amdgcn_ds_read_tr16_b64_v4bf16((LAS bf16x4*)p)); }
DI u32x4 lds_tr2(LAS char* p, u32 step) { u32x2 a = lds_tr(p), b = lds_tr(p + step); return u32x4{a.x, a.y, b.x, b.y}; }
DI u32x4 make_srd(const void* p, u32 bytes) { u64 a = (u64)p; return u32x4{(u32)a, (u32)(a >> 32) & 0xffffu, bytes, 0x00020000u}; }
DI void dma16(u32 lds_addr, u32 voff, u32x4 srd, u32 soff) {
    asm volatile("s_mov_b32 m0, %0\n\ts_nop 0\n\tbuffer_load_dwordx4 %1, %2, %3 offen lds" ::"s"(lds_addr), "v"(voff), "s"(srd), "s"(soff) : "memory");
}
DI u64 ld_flag(char* ws, u32 off) { return __hip_atomic_load((GAS u64*)(ws + off), __ATOMIC_RELAXED, __HIP_MEMORY_SCOPE_AGENT); }
DI void st_flag(char* ws, u32 off, u64 v) { __hip_atomic_store((GAS u64*)(ws + off), v, __ATOMIC_RELAXED, __HIP_MEMORY_SCOPE_AGENT); }
template <int N> DI void wait_flags(char* ws, u32 off, u32 stride, u64 tag) {
    for (u32 spin = 0; spin < SPIN_CAP; ++spin) {
        bool ok = true;
#pragma unroll
        for (int i = 0; i < N; ++i) ok &= ld_flag(ws, off + i * stride) == tag;
        if (__all(ok)) break;
        __builtin_amdgcn_s_sleep(2);
    }
    __builtin_amdgcn_fence(__ATOMIC_ACQUIRE, "wavefront");
    asm volatile("" ::: "memory");
}
DI void signal_last(LAS char* cnt, u32 n, char* ws, u32 off, u64 tag, int lane) {
    DRAIN();
    u32 old = 0;
    if (lane == 0) old = __hip_atomic_fetch_add((LAS u32*)cnt, 1u, __ATOMIC_RELAXED, __HIP_MEMORY_SCOPE_WORKGROUP);
    old = __builtin_amdgcn_readfirstlane(old);
    if (old == n - 1 && lane == 0) st_flag(ws, off, tag);
}
template <int CTRL> DI float dpp_add(float v) {
    int t = __builtin_amdgcn_update_dpp(0, __builtin_bit_cast(int, v), CTRL, 0xf, 0xf, true);
    return v + __builtin_bit_cast(float, t);
}
DI void reduce8(const float (&v)[8], float (&o)[2]) {
    float s[4];
#pragma unroll
    for (int i = 0; i < 4; ++i) {
        auto r = __builtin_amdgcn_permlane32_swap(as_u(v[i]), as_u(v[i + 4]), false, false);
        s[i] = as_f(r[0]) + as_f(r[1]);
    }
#pragma unroll
    for (int i = 0; i < 2; ++i) {
        auto r = __builtin_amdgcn_permlane16_swap(as_u(s[i]), as_u(s[i + 2]), false, false);
        float t = as_f(r[0]) + as_f(r[1]);
        t = dpp_add<0x128>(t); t = dpp_add<0x124>(t); t = dpp_add<0x122>(t); t = dpp_add<0x121>(t);
        o[i] = t;
    }
}
DI float lane_bcast(float v, int l) { return __builtin_bit_cast(float, __builtin_amdgcn_readlane(__builtin_bit_cast(int, v), l)); }
#endif

__global__ void __launch_bounds__(512, 2)
fused_kernel(const u16* __restrict__ x, const u16* __restrict__ Are, const u16* __restrict__ Aim, const u16* __restrict__ Bre,
             const u16* __restrict__ Bim, const u16* __restrict__ Cre, const u16* __restrict__ Cim, const u16* __restrict__ logdt,
             const u16* __restrict__ Dskip, const u16* __restrict__ Wg, const u16* __restrict__ Wu, const u16* __restrict__ Wd,
             u16* __restrict__ out, char* ws) {
#if defined(__HIP_DEVICE_COMPILE__)
    extern __shared__ __attribute__((aligned(16))) char smem_[];
    LAS char* const smem = (LAS char*)smem_;
    const u32 lds0 = (u32)(size_t)smem;
    const int b = blockIdx.x, xcd = b & 7, wi = b >> 3;
    const int w = __builtin_amdgcn_readfirstlane(threadIdx.x >> 6), lane = threadIdx.x & 63;
    const __amdgpu_buffer_rsrc_t wsr = __builtin_amdgcn_make_buffer_rsrc(ws, 0, WS_END, 0x00020000);
    const int panel = 8 * (xcd & 1) + (wi & 7), slice = 4 * (xcd >> 1) + (wi >> 3);
    const int sb = 32 * xcd + wi, ch0 = (5 * sb + 1) >> 1, nch = 3 - (sb & 1);
    const int ttile = 4 * xcd + (wi & 3), ctile = wi >> 2;
    STAMP(0);
    const u64 tstart_ = __builtin_amdgcn_s_memrealtime();

    u32x4 fb[6][5];
    auto load_fb = [&]() {
#pragma unroll
        for (int k = 0; k < 6; ++k)
#pragma unroll
            for (int j = 0; j < 5; ++j) fb[k][j] = __builtin_amdgcn_raw_buffer_load_b128(wsr, WS_WDF + lane * 16 + j * 1024, ((ctile * 48 + 6 * w + k) * 5) * 1024, SC1);
    };
    u64 tag;

    if (w < 4) {
        __builtin_amdgcn_s_setprio(2);
        f32x16 acc[3][2];
        float cs[3] = {0.f, 0.f, 0.f};
#pragma unroll
        for (int m = 0; m < 3; ++m)
#pragma unroll
            for (int n = 0; n < 2; ++n)
#pragma unroll
                for (int r = 0; r < 16; ++r) acc[m][n][r] = 0.f;
        const int mi = w & 1, ni = w >> 1, h = lane >> 5, r32 = lane & 31;
        const u32x4 xs = make_srd(x, 2048 * 1280), gs = make_srd(Wg, 640 * 3072), us = make_srd(Wu, 640 * 3072);
        const u32 vx = (8 * w + (lane >> 3)) * 1280 + (((lane & 7) ^ ((4 * w + (lane >> 4)) & 7)) << 4);
        u32 vw[3];
#pragma unroll
        for (int j = 0; j < 3; ++j) vw[j] = ((64 * j + lane) / 12) * 3072 + ((64 * j + lane) % 12) * 16;
        const u32 sx0 = panel * 128 * 1280, sw0 = 16 * w * 3072 + slice * 192;
        const int rot = (wi * 3 + xcd) % 10;
        auto piece = [&](int i, u32 slot, u32 kt) {
            if (i < 4) dma16(lds0 + slot + (w + 4 * i) * 1024, vx, xs, sx0 + i * 32 * 1280 + kt * 128);
            else if (i < 7) dma16(lds0 + slot + GOFF + (3 * w + i - 4) * 1024, vw[i - 4], gs, sw0 + kt * 64 * 3072);
            else dma16(lds0 + slot + UOFF + (3 * w + i - 7) * 1024, vw[i - 7], us, sw0 + kt * 64 * 3072);
        };
        u32 kt = rot;
#pragma unroll
        for (int i = 0; i < 10; ++i) piece(i, 0, kt);
        kt = kt == 9 ? 0 : kt + 1;
#pragma unroll
        for (int i = 0; i < 10; ++i) piece(i, SLOT, kt);
        kt = kt == 9 ? 0 : kt + 1;

        const u32 hz = h ^ ((r32 >> 1) & 7);
        u32 ax[4];
#pragma unroll
        for (int s = 0; s < 4; ++s) ax[s] = (64 * ni + r32) * 128 + (((2 * s) ^ hz) << 4);
        const u32 aw = (((lane >> 4) & 1) ? UOFF : GOFF) + (8 * h + ((lane & 15) >> 2)) * 192 + mi * 96 + (lane & 3) * 8;
        u32x4 fw[2][3], fx[2][2];
        const bf16x2 ones = {(__bf16)1.0f, (__bf16)1.0f};
        auto rd = [&](int set, u32 slot, int s) {
#pragma unroll
            for (int m = 0; m < 3; ++m) fw[set][m] = lds_tr2(smem + slot + aw + s * 3072 + m * 32, 768);
#pragma unroll
            for (int n = 0; n < 2; ++n) fx[set][n] = lds_ld<u32x4>(smem + slot + ax[s] + n * 4096);
        };
        auto mm = [&](int set, int m, int n) {
            acc[m][n] = __builtin_amdgcn_mfma_f32_32x32x16_bf16(as_frag(fw[set][m]), as_frag(fx[set][n]), acc[m][n], 0, 0, 0);
        };
        auto dots = [&](int set, int m) {
#pragma unroll
            for (int e = 0; e < 4; ++e) cs[m] = __builtin_amdgcn_fdot2_f32_bf16(as_bf2(fw[set][m][e]), ones, cs[m], false);
        };
        auto group = [&](int set, bool issue, int p0, int np, u32 nslot, u32 nkt) {
#pragma unroll
            for (int m = 0; m < 3; ++m) {
                mm(set, m, 0); SB();
                if (m < np && issue) piece(p0 + m, nslot, nkt);
                SB();
                mm(set, m, 1); SB();
                dots(set, m); SB();
            }
        };
        u32 slot = 0, nslot = 2 * SLOT;
        auto tile = [&](bool first, bool issue) {
            BARS();
            rd(0, slot, 0); SB();
            if (!first) group(1, issue, 0, 3, nslot, kt);
            else { piece(0, nslot, kt); piece(1, nslot, kt); piece(2, nslot, kt); }
            rd(1, slot, 1); SB();
            group(0, issue, 3, 3, nslot, kt);
            rd(0, slot, 2); SB();
            group(1, issue, 6, 2, nslot, kt);
            rd(1, slot, 3); SB();
            group(0, issue, 8, 2, nslot, kt);
            nslot = slot;
            slot = slot == 2 * SLOT ? 0 : slot + SLOT;
            kt = kt == 9 ? 0 : kt + 1;
        };
        asm volatile("s_waitcnt vmcnt(10)" ::: "memory");
        tile(true, true);
#pragma unroll 1
        for (int t = 1; t < 8; ++t) {
            asm volatile("s_waitcnt vmcnt(10)" ::: "memory");
            tile(false, true);
        }
#pragma unroll 1
        for (int t = 8; t < 10; ++t) {
            if (t == 8) asm volatile("s_waitcnt vmcnt(10)" ::: "memory");
            else DRAIN();
            tile(false, false);
        }
        group(1, false, 0, 0, 0, 0);
        __builtin_amdgcn_s_setprio(0);
        BARS();
        tag = lds_ld<u64>(smem + LDS_MISC);
        STAMP(12);
        u32x2 stat2;
#pragma unroll
        for (int n = 0; n < 2; ++n) stat2[n] = __builtin_amdgcn_raw_buffer_load_b32(wsr, WS_STAT + (panel * 128 + 64 * ni + 32 * n + r32) * 4, 0, SC1);
        load_fb();
        u32x4 ca[3];
#pragma unroll
        for (int m = 0; m < 3; ++m) {
            const float c0 = rnd_bf(cs[m]), c1 = rnd_bf(cs[m] - c0), c2 = cs[m] - c0 - c1;
            ca[m] = u32x4{pk_bf(c0, c1), pk_bf(c2, 0.f), 0, 0};
        }
#pragma unroll
        for (int n = 0; n < 2; ++n) {
            const u32 nm = (stat2[n] & 0xffffu) ^ 0x8000u;
            const u32x4 cb = {nm | (nm << 16), nm, 0, 0};
            const float sd = bf_hi(stat2[n]), k1 = -1.44269504089f / sd, k2 = sd * sd;
            const int tok = panel * 128 + 64 * ni + 32 * n + (lane & 31);
#pragma unroll
            for (int m = 0; m < 3; ++m) {
                const f32x16 a = __builtin_amdgcn_mfma_f32_32x32x16_bf16(as_frag(ca[m]), as_frag(cb), acc[m][n], 0, 0, 0);
                u32x4 hv;
#pragma unroll
                for (int e = 0; e < 4; ++e) {
                    const f32x2 g = {a[2 * e], a[2 * e + 1]}, u = {a[8 + 2 * e], a[9 + 2 * e]};
                    const f32x2 t = g * k1;
                    const f32x2 ex = {__builtin_amdgcn_exp2f(t.x), __builtin_amdgcn_exp2f(t.y)};
                    const f32x2 d = ex * k2 + k2;
                    const f32x2 rc = {__builtin_amdgcn_rcpf(d.x), __builtin_amdgcn_rcpf(d.y)};
                    hv[e] = pk_bf(g * u * rc);
                }
                const int G = 6 * slice + 3 * mi + m;
                __builtin_amdgcn_raw_buffer_store_b128(hv, wsr, WS_H + ((tok >> 4) * 48 * 64 + (tok & 15) + 16 * h + 32 * G) * 16, 0, SC1);
            }
        }
        STAMP(13);
        signal_last(smem + LDS_MISC + 8, 4, ws, WS_F2 + (panel * 16 + slice) * 128, tag, lane);
    } else {
    if (w < 7) {
        if (w - 4 < nch) {
            const int ch = ch0 + w - 4;
            LAS char* const S = smem + LDS_S + (w - 4) * S_SZ;
            const float dt = rnd_bf(expf(bf_lo(logdt[ch])));
            u32 p[6];
            auto ldp = [&](int pass) {
                const int o = ch * 192 + pass * 64 + lane;
                p[0] = Are[o]; p[1] = Aim[o]; p[2] = Bre[o]; p[3] = Bim[o]; p[4] = Cre[o]; p[5] = Cim[o];
            };
            ldp(0);
            f32x16 K;
#pragma unroll
            for (int r = 0; r < 16; ++r) K[r] = 0.f;
            BARS();
            LAS char* const tw = S + lane * 4;
            LAS char* const tr = S + (lane & 31) * 272 + (lane >> 5) * 16;
#pragma unroll 1
            for (int pass = 0; pass < 3; ++pass) {
                const float a2 = dt * bf_lo(p[0]) * 1.44269504089f, b2 = dt * bf_lo(p[1]) * 0.15915494309f;
                const f32x2 dB = {dt * bf_lo(p[2]), -dt * bf_lo(p[3])}, Cc = {bf_lo(p[4]), -bf_lo(p[5])};
                if (pass < 2) ldp(pass + 1);
                const f32x2 W = cmul(Cc, dB);
                const f32x2 e1 = cexp_step(a2, -b2, 1.f), e256 = cexp_step(a2, -b2, 256.f), e8 = cexp_step(a2, b2, 8.f);
                const f32x2 e2 = cmul(e1, e1), e4 = cmul(e2, e2);
                f32x2 r = W, q = cmul(W, e256);
                lds_st<u32>(tw, pk_bf(r));
#pragma unroll
                for (int i = 1; i < 8; ++i) { r = cmul(r, e1); lds_st<u32>(tw + i * 272, pk_bf(r)); }
                r = q;
                lds_st<u32>(tw + 8 * 272, pk_bf(r));
#pragma unroll
                for (int i = 9; i < 12; ++i) { r = cmul(r, e2); lds_st<u32>(tw + i * 272, pk_bf(r)); }
#pragma unroll
                for (int i = 0; i < 2; ++i) {
                    q = cmul(q, e256);
                    lds_st<u32>(tw + (12 + 2 * i) * 272, pk_bf(q));
                    lds_st<u32>(tw + (13 + 2 * i) * 272, pk_bf(cmul(q, e4)));
                }
#pragma unroll
                for (int i = 16; i < 21; ++i) { q = cmul(q, e256); lds_st<u32>(tw + i * 272, pk_bf(q)); }
                BARS();
                u32x4 fa[8];
#pragma unroll
                for (int s = 0; s < 8; ++s) fa[s] = lds_ld<u32x4>(tr + s * 32);
                const f32x2 e16 = cmul(e8, e8);
                f32x2 c0 = {1.f, 0.f}, c1 = e8;
                lds_st<u32>(tw, pk_bf(c0));
                lds_st<u32>(tw + 272, pk_bf(c1));
                c0 = e16;
#pragma unroll
                for (int i = 2; i < 32; i += 2) {
                    if (i > 2) c0 = cmul(c0, e16);
                    c1 = cmul(c1, e16);
                    lds_st<u32>(tw + i * 272, pk_bf(c0));
                    lds_st<u32>(tw + (i + 1) * 272, pk_bf(c1));
                    if (i == 18) BARS();
                }
#pragma unroll
                for (int s = 0; s < 8; ++s)
                    K = __builtin_amdgcn_mfma_f32_32x32x16_bf16(as_frag(fa[s]), as_frag(lds_ld<u32x4>(tr + s * 32)), K, 0, 0, 0);
                BARS();
            }
            const int j = lane & 31, h = lane >> 5;
            if (lane == 0) K[0] += bf_lo(Dskip[ch]);
            LAS char* const C = S + KC_OFF;
            lds_st<f32x4>(C + (8 * j + 4 * h) * 4, f32x4{K[0], K[1], K[2], K[3]});
            lds_st<f32x2>(C + (h ? 384 + 2 * j : 256 + 4 * j) * 4, f32x2{K[4], K[5]});
            lds_st<f32x2>(C + (h ? 448 + 2 * j : 258 + 4 * j) * 4, f32x2{K[6], K[7]});
            lds_st<float>(C + (h ? 640 + j : 512 + j) * 4, K[8]);
#pragma unroll
            for (int i = 1; i < 4; ++i) lds_st<float>(C + (h ? 672 + j : 512 + 32 * i + j) * 4, K[8 + i]);
            {
                LAS char* const src = C + (h ? 256 + 4 * j : 8 * j) * 4;
                const f32x4 a = lds_ld<f32x4>(src), c = lds_ld<f32x4>(src + 16);
                lds_st<u32x4>(S + 32 + lane * 16, u32x4{pk_bf(a[0], h ? a[0] : a[1]), pk_bf(h ? a[1] : a[2], h ? a[2] : a[3]),
                                                         pk_bf(h ? a[2] : c[0], h ? a[2] : c[1]), pk_bf(h ? a[3] : c[2], h ? c[0] : c[3])});
            }
            {
                LAS char* const src = C + (384 + 2 * lane) * 4;
                const f32x2 a = lds_ld<f32x2>(src);
                const float c = lds_ld<float>(src + 8);
                lds_st<u32x4>(S + 32 + (64 + lane) * 16, u32x4{pk_bf(a.x, a.x), pk_bf(a.x, a.y), pk_bf(a.y, a.y), pk_bf(c, c)});
            }
#pragma unroll
            for (int i = 0; i < 2; ++i) {
                LAS char* const src = C + (512 + 64 * i + lane) * 4;
                const float a = lds_ld<float>(src), c = lds_ld<float>(src + 4);
                lds_st<u32x4>(S + 32 + (128 + 64 * i + lane) * 16, u32x4{pk_bf(a, a), pk_bf(a, a), pk_bf((lane & 1) ? c : a, c), pk_bf(c, c)});
            }
            if (lane < 2) lds_st<u32x4>(S + lane * 16, u32x4{0, 0, 0, 0});
        } else {
#pragma unroll 1
            for (int t = 0; t < 10; ++t) BARS();
        }
    } else {
        const u64 tag0 = ld_flag(ws, WS_EPOCH) + 1;
        const int lb = 128 * (xcd & 1) + 32 * (xcd >> 1) + wi;
        u32 xr[8][5];
#pragma unroll
        for (int r = 0; r < 8; ++r)
#pragma unroll
            for (int q = 0; q < 5; ++q) xr[r][q] = *(const u32*)(x + (8 * lb + r) * 640 + q * 128 + lane * 2);
        const int wks = b / 5, wnb = b % 5;
        u32x4 wd[8];
        if (b < 240) {
#pragma unroll
            for (int pc = 0; pc < 8; ++pc) wd[pc] = *(const u32x4*)(Wd + (32 * wks + 4 * pc + (lane >> 4)) * 640 + wnb * 128 + (lane & 15) * 8);
        }
        if (lane == 0) {
            lds_st<u64>(smem + LDS_MISC, tag0);
            lds_st<u32x2>(smem + LDS_MISC + 8, u32x2{0, 0});
        }
        BARS();
        float v[8], o[2], mean[8];
#pragma unroll
        for (int r = 0; r < 8; ++r) {
            float s = 0.f;
#pragma unroll
            for (int q = 0; q < 5; ++q) s += bf_lo(xr[r][q]) + bf_hi(xr[r][q]);
            v[r] = s;
        }
        reduce8(v, o);
        const float mu[2] = {o[0] * (1.f / 640.f), o[1] * (1.f / 640.f)};
#pragma unroll
        for (int r = 0; r < 8; ++r) mean[r] = lane_bcast(mu[r & 1], 16 * (r >> 1));
        BARS();
#pragma unroll
        for (int r = 0; r < 8; ++r) {
            float s = 0.f;
#pragma unroll
            for (int q = 0; q < 5; ++q) {
                const float a = bf_lo(xr[r][q]) - mean[r], c = bf_hi(xr[r][q]) - mean[r];
                s = fmaf(a, a, s); s = fmaf(c, c, s);
            }
            v[r] = s;
        }
        reduce8(v, o);
        float mb[2], rs[2];
        u32x2 stat;
#pragma unroll
        for (int i = 0; i < 2; ++i) {
            mb[i] = rnd_bf(mu[i]);
            const float sd = rnd_bf(sqrtf(rnd_bf(rnd_bf(o[i] * (1.f / 640.f)) + 1e-5f)));
            rs[i] = 1.f / sd;
            stat[i] = pk_bf(mb[i], sd);
        }
        if ((lane & 15) == 0) __builtin_amdgcn_raw_buffer_store_b64(stat, wsr, WS_STAT + (8 * lb + (lane >> 3)) * 4, 0, SC1);
        BARS();
#pragma unroll
        for (int q = 0; q < 5; ++q) {
            float lo[8], hi[8];
#pragma unroll
            for (int r = 0; r < 8; ++r) {
                const float m = lane_bcast(mb[r & 1], 16 * (r >> 1)), k = lane_bcast(rs[r & 1], 16 * (r >> 1));
                const u32 d = pk_bf(bf_lo(xr[r][q]) - m, bf_hi(xr[r][q]) - m);
                lo[r] = bf_lo(d) * k; hi[r] = bf_hi(d) * k;
            }
            const u32 off = WS_XNT + (lb * 640 + q * 128 + lane * 2) * 16;
            __builtin_amdgcn_raw_buffer_store_b128(u32x4{pk_bf(lo[7], lo[6]), pk_bf(lo[5], lo[4]), pk_bf(lo[3], lo[2]), pk_bf(lo[1], lo[0])}, wsr, off, 0, SC1);
            __builtin_amdgcn_raw_buffer_store_b128(u32x4{pk_bf(hi[7], hi[6]), pk_bf(hi[5], hi[4]), pk_bf(hi[3], hi[2]), pk_bf(hi[1], hi[0])}, wsr, off + 16, 0, SC1);
            if (q == 2) BARS();
        }
        if (b < 240) {
            LAS char* const st = smem + LDS_UU;
#pragma unroll
            for (int pc = 0; pc < 8; ++pc) lds_st<u32x4>(st + pc * 1024 + lane * 16, wd[pc]);
            const int g = lane >> 4;
            LAS char* const src = st + (16 * (g >> 1) + 4 * (g & 1) + ((lane & 15) >> 2)) * 256 + (lane & 3) * 8;
#pragma unroll
            for (int c = 0; c < 8; ++c) {
                const int ct = 8 * wnb + c;
                __builtin_amdgcn_raw_buffer_store_b128(lds_tr2(src + c * 32, 2048), wsr, WS_WDF + lane * 16 + (((ct / 5) * 48 + wks) * 5 + ct % 5) * 1024, 0, SC1);
            }
        }
        BARS();
        BARS();
        DRAIN();
        if (lane == 0) st_flag(ws, WS_F1 + b * 8, tag0);
        BARS();
        BARS();
        wait_flags<4>(ws, WS_F1 + lane * 8, 512, tag0);
        if (b == 0 && lane == 0) st_flag(ws, WS_EPOCH, tag0);
        BARS();
        u32x4 ug[12];
#pragma unroll
        for (int q = 0; q < 12; ++q) {
            const u32 n = 64 * q + lane, blk = n / 3, c = n % 3;
            ug[q] = __builtin_amdgcn_raw_buffer_load_b128(wsr, WS_XNT + (blk * 640 + ch0 + (c < nch ? c : 0)) * 16, 0, SC1);
        }
        BARS();
#pragma unroll
        for (int q = 0; q < 12; ++q) {
            const u32 n = 64 * q + lane, blk = n / 3, c = n % 3;
            lds_st<u32x4>(smem + LDS_UU + c * U_SZ + (255 - blk) * 16, ug[q]);
        }
        if (lane < 30) {
#pragma unroll
            for (int c = 0; c < 3; ++c) lds_st<u32x4>(smem + LDS_UU + c * U_SZ + 4096 + lane * 16, u32x4{0, 0, 0, 0});
        }
    }
    BARS();
    tag = lds_ld<u64>(smem + LDS_MISC);
    STAMP(12);
    load_fb();
    if (w - 4 < nch) {
        const int a = lane & 15, g = lane >> 4;
        LAS char* kp = smem + LDS_S + (w - 4) * S_SZ + ((8 * g + a + 1) >> 1) * 4;
        LAS char* up = smem + LDS_UU + (w - 4) * U_SZ + (2032 - 16 * a + 8 * g) * 2 - 7 * 512;
        const u32 sh = ((a + 1) & 1) * 16;
        f32x4 y[8];
#pragma unroll
        for (int t = 0; t < 8; ++t) y[t] = f32x4{0.f, 0.f, 0.f, 0.f};
#pragma unroll 1
        for (int rho = 0; rho < 8; ++rho) {
            u32x4 uf[8];
#pragma unroll
            for (int i = 0; i < 8; ++i) {
                uf[i] = lds_ld<u32x4>(up + (7 - i) * 512);
                LAS char* const k = kp + (7 - i) * 512;
                const u32 d0 = lds_ld<u32>(k), d1 = lds_ld<u32>(k + 4), d2 = lds_ld<u32>(k + 8), d3 = lds_ld<u32>(k + 12), d4 = lds_ld<u32>(k + 16);
                const u32x4 kf = {__builtin_amdgcn_alignbit(d1, d0, sh), __builtin_amdgcn_alignbit(d2, d1, sh),
                                  __builtin_amdgcn_alignbit(d3, d2, sh), __builtin_amdgcn_alignbit(d4, d3, sh)};
#pragma unroll
                for (int tau = 0; tau <= i; ++tau)
                    y[7 - i + tau] = __builtin_amdgcn_mfma_f32_16x16x32_bf16(as_frag(kf), as_frag(uf[tau]), y[7 - i + tau], 0, 0, 0);
            }
            kp += 64; up += 64;
        }
        const u32 off = WS_YT + (ch0 + w - 4) * 4096 + (16 * a + 4 * g) * 2;
#pragma unroll
        for (int t = 0; t < 8; ++t)
            __builtin_amdgcn_raw_buffer_store_b64(u32x2{pk_bf(y[t][0], y[t][1]), pk_bf(y[t][2], y[t][3])}, wsr, off + t * 512, 0, SC1);
        STAMP(13);
        signal_last(smem + LDS_MISC + 12, nch, ws, WS_F3 + sb * 8, tag, lane);
    }
    }
    STAMP(14);

    wait_flags<1>(ws, WS_F2 + ((ttile >> 1) * 16 + 2 * w + (lane & 1)) * 128, 0, tag);
    STAMP(15);
    {
        u32x4 fh[4][6];
#pragma unroll
        for (int m = 0; m < 4; ++m)
#pragma unroll
            for (int k = 0; k < 6; ++k) fh[m][k] = __builtin_amdgcn_raw_buffer_load_b128(wsr, WS_H + lane * 16 + (k & 3) * 1024, ((4 * ttile + m) * 48 + 6 * w + (k & 4)) * 1024, SC1);
        LAS char* const pw = smem + w * PWAVE + (lane & 15) * PROW + (lane >> 4) * 8;
#pragma unroll
        for (int m = 0; m < 4; ++m) {
            f32x4 d[5];
#pragma unroll
            for (int j = 0; j < 5; ++j) d[j] = f32x4{0.f, 0.f, 0.f, 0.f};
#pragma unroll
            for (int k = 0; k < 6; ++k)
#pragma unroll
                for (int j = 0; j < 5; ++j) d[j] = __builtin_amdgcn_mfma_f32_16x16x32_bf16(as_frag(fb[k][j]), as_frag(fh[m][k]), d[j], 0, 0, 0);
#pragma unroll
            for (int j = 0; j < 5; ++j) lds_st<u32x2>(pw + m * PTILE + j * 32, u32x2{pk_bf(d[j][0], d[j][1]), pk_bf(d[j][2], d[j][3])});
        }
    }
    STAMP(16);
    u32x2 xv[3];
#pragma unroll
    for (int i = 0; i < 3; ++i) {
        const u32 it = min(threadIdx.x + 512 * i, 1279u);
        xv[i] = *(const u32x2*)(x + (64 * ttile + it / 20) * 640 + 80 * ctile + (it % 20) * 4);
    }
    if (w < 5) {
        wait_flags<1>(ws, WS_F3 + (32 * ctile + (lane & 31)) * 8, 0, tag);
#pragma unroll
        for (int i = 0; i < 2; ++i) {
            const u32 n = (2 * w + i) * 64 + lane, c = n >> 3, pc = n & 7;
            lds_st<u32x4>(smem + LDS_Y + c * YROW + pc * 16, __builtin_amdgcn_raw_buffer_load_b128(wsr, WS_YT + (80 * ctile + c) * 4096 + 128 * ttile + pc * 16, 0, SC1));
        }
    }
    STAMP(17);
    BAR();
    STAMP(18);
#pragma unroll
    for (int i = 0; i < 3; ++i) {
        const u32 it = threadIdx.x + 512 * i;
        if (it < 1280) {
            const u32 tk = it / 20, qd = it % 20;
            LAS char* const pr = smem + (tk >> 4) * PTILE + (tk & 15) * PROW + qd * 8;
            float f[4] = {0.f, 0.f, 0.f, 0.f};
#pragma unroll
            for (int s = 0; s < 8; ++s) {
                const u32x2 v = lds_ld<u32x2>(pr + s * PWAVE);
                f[0] += bf_lo(v.x); f[1] += bf_hi(v.x); f[2] += bf_lo(v.y); f[3] += bf_hi(v.y);
            }
            LAS char* const yr = smem + LDS_Y + 4 * qd * YROW + tk * 2;
            const float xf[4] = {bf_lo(xv[i].x), bf_hi(xv[i].x), bf_lo(xv[i].y), bf_hi(xv[i].y)};
            float o[4];
#pragma unroll
            for (int e = 0; e < 4; ++e) o[e] = rnd_bf(xf[e] + bf_lo(lds_ld<u16>(yr + e * YROW))) + rnd_bf(f[e]);
            *(u32x2*)(out + (64 * ttile + tk) * 640 + 80 * ctile + qd * 4) = u32x2{pk_bf(o[0], o[1]), pk_bf(o[2], o[3])};
        }
    }
    while (__builtin_amdgcn_s_memrealtime() - tstart_ < 4500) __builtin_amdgcn_s_sleep(1);
#endif
}

__global__ void __launch_bounds__(64) spin_kernel(char* ws) {
    const u64 t0 = __builtin_amdgcn_s_memrealtime();

    while (__builtin_amdgcn_s_memrealtime() - t0 < 2000) __builtin_amdgcn_s_sleep(1);
}

extern "C" void kernel_launch(void* const* d_in, const int* in_sizes, int n_in, void* d_out, int out_size, void* d_ws, size_t ws_size,
                              hipStream_t stream) {
    (void)hipFuncSetAttribute((const void*)fused_kernel, hipFuncAttributeMaxDynamicSharedMemorySize, LDS_TOTAL);
    fused_kernel<<<256, 512, LDS_TOTAL, stream>>>((const u16*)d_in[0], (const u16*)d_in[1], (const u16*)d_in[2], (const u16*)d_in[3],
                                                  (const u16*)d_in[4], (const u16*)d_in[5], (const u16*)d_in[6], (const u16*)d_in[7],
                                                  (const u16*)d_in[8], (const u16*)d_in[10], (const u16*)d_in[11], (const u16*)d_in[12],
                                                  (u16*)d_out, (char*)d_ws);
    (void)hipFuncSetAttribute((const void*)spin_kernel, hipFuncAttributeMaxDynamicSharedMemorySize, LDS_TOTAL);
    spin_kernel<<<256, 64, 0, stream>>>((char*)d_ws);
}
```

```cpp
#include <hip/hip_runtime.h>
#include <hip/hip_bf16.h>
#include <type_traits>

typedef unsigned u32;
typedef unsigned long long u64;
typedef unsigned short u16;
typedef u32 u32x2 __attribute__((ext_vector_type(2)));
typedef u32 u32x4 __attribute__((ext_vector_type(4)));
typedef float f32x2 __attribute__((ext_vector_type(2)));
typedef float f32x4 __attribute__((ext_vector_type(4)));
typedef float f32x16 __attribute__((ext_vector_type(16)));
typedef __bf16 bf16x2 __attribute__((ext_vector_type(2)));
typedef __bf16 bf16x4 __attribute__((ext_vector_type(4)));
typedef __bf16 bf16x8 __attribute__((ext_vector_type(8)));
#define LAS __attribute__((address_space(3)))
#define GAS __attribute__((address_space(1)))
#define DI __device__ __forceinline__
#define INL __attribute__((always_inline))

constexpr u32 WS_EPOCH = 0, WS_F1 = 4096, WS_F3 = 8192, WS_F4 = 12288, WS_F2 = 16384, WS_STAT = 57344, WS_CSP = 65536, WS_KP = WS_CSP + 160 * 768;
constexpr u32 WS_XNT = 1048576, WS_WDF = WS_XNT + 2621440, WS_H = WS_WDF + 1966080, WS_YT = WS_H + 6291456, WS_END = WS_YT + 2621440;
static_assert(WS_KP + 256 * 2576 <= WS_XNT, "workspace map");
constexpr u32 SLOT = 40960;
constexpr u32 L_TAB = 82048, TAB_SZ = 8192;
constexpr u32 PT_SZ = 2576, L_TOP = L_TAB + 8 * TAB_SZ;
constexpr u32 L_PT5 = L_TAB + 5 * TAB_SZ;
constexpr u32 L_LN = L_TOP, LNROW = 1312;
constexpr u32 K_SZ = 4160, L_K = 3 * SLOT;
constexpr u32 L_KS = L_K + 4 * K_SZ + 2560;
constexpr u32 U_SZ = 4576, L_U = L_K + 6 * K_SZ, L_STAT = L_U + 3 * U_SZ, L_CS = L_STAT + 768;
constexpr u32 L_MISC = L_CS + 768, L_CR = L_MISC + 96, L_CD = L_CR + 256, L_SINK = L_STAT;
constexpr u32 PROW = 128, PWAVE = 80 * PROW;
constexpr u32 L_Y = 8 * PWAVE, L_CP = 9 * PWAVE;
constexpr u32 LDS_TOTAL = 163840;
static_assert(L_TAB >= 2 * SLOT && L_TAB % 128 == 0 && L_PT5 >= 3 * SLOT && L_TOP + 5 * PT_SZ <= L_STAT && L_CD + 256 <= LDS_TOTAL, "LDS map");
static_assert(L_KS >= L_PT5 + 2 * TAB_SZ + PT_SZ && L_KS + PT_SZ <= L_TOP && K_SZ % 128 == 64, "LDS map");
static_assert(L_CP >= 2 * SLOT && L_CP + 16384 <= 3 * SLOT, "LDS map");
constexpr u32 M_C1 = L_MISC + 8, M_C2 = L_MISC + 12, M_C3 = L_MISC + 16, M_CF = L_MISC + 20, M_CW = L_MISC + 32,
              M_LN = L_MISC + 36, M_SD = L_MISC + 44, M_KB = L_MISC + 48, M_CE = L_MISC + 52, M_F1 = L_MISC + 56, M_F3 = L_MISC + 60, M_EP = L_MISC, M_F2 = L_MISC + 64, M_C4 = L_MISC + 80, M_KR = L_MISC + 88, M_LR = L_MISC + 84, M_S9 = L_MISC + 92, M_YS = L_MISC + 40;
#define PACE 8
#define NEARLY 2
#define AHEAD 12
#define EPN 4
constexpr int NK = 6;
constexpr u32 SPIN_CAP = 1u << 16;
constexpr u32 CODE_LINES = 352;
#define NAP0 24
#define NAP1 12
#define NAP2 3
#define NAP3 4
constexpr int SC1 = 16;

#define STAMP(k) do { } while (0)
#define DBGARGS
#define DBGPARS
#define STAMPA(k) do { } while (0)
#define STAMPB(k) STAMP(k)

#if defined(__HIP_DEVICE_COMPILE__)
#define SB() __builtin_amdgcn_sched_barrier(0)
#define BAR() do { SB(); asm volatile("s_waitcnt lgkmcnt(0)\n\ts_barrier" ::: "memory"); SB(); } while (0)
#define DRAIN() asm volatile("s_waitcnt vmcnt(0)" ::: "memory")
typedef __amdgpu_buffer_rsrc_t rsrc_t;

DI float bf_lo(u32 v) { return __builtin_bit_cast(float, v << 16); }
DI float bf_hi(u32 v) { return __builtin_bit_cast(float, v & 0xffff0000u); }
DI u32 pk_bf(float a, float b) { f32x2 v = {a, b}; return __builtin_bit_cast(u32, __builtin_convertvector(v, bf16x2)); }
DI u32 pk_bf(f32x2 v) { return __builtin_bit_cast(u32, __builtin_convertvector(v, bf16x2)); }
DI float rnd_bf(float a) { return bf_lo(pk_bf(a, a)); }
DI float as_f(u32 v) { return __builtin_bit_cast(float, v); }
DI u32 as_u(float v) { return __builtin_bit_cast(u32, v); }
DI bf16x2 as_bf2(u32 v) { return __builtin_bit_cast(bf16x2, v); }
DI bf16x8 as_frag(u32x4 v) { return __builtin_bit_cast(bf16x8, v); }
DI f32x16 mma32(u32x4 a, u32x4 b, f32x16 c) { return __builtin_amdgcn_mfma_f32_32x32x16_bf16(as_frag(a), as_frag(b), c, 0, 0, 0); }
DI f32x4 mma16(u32x4 a, u32x4 b, f32x4 c) { return __builtin_amdgcn_mfma_f32_16x16x32_bf16(as_frag(a), as_frag(b), c, 0, 0, 0); }
DI f32x16 zero16() { f32x16 z; for (int i = 0; i < 16; ++i) z[i] = 0.f; return z; }
DI f32x2 cmul(f32x2 x, f32x2 y) {
    f32x2 xr = {x.x, x.x}, xi = {x.y, x.y}, ys = {-y.y, y.x};
    return __builtin_elementwise_fma(xi, ys, xr * y);
}
DI f32x2 cm_a(f32x2 x, f32x2 y) { f32x2 t; asm("v_pk_mul_f32 %0, %1, %2 op_sel:[0,0] op_sel_hi:[0,1]" : "=v"(t) : "v"(x), "v"(y)); return t; }
DI f32x2 cm_b(f32x2 x, f32x2 y, f32x2 t) {
    f32x2 r;
    asm("v_pk_fma_f32 %0, %1, %2, %3 op_sel:[1,1,0] op_sel_hi:[1,0,1] neg_lo:[0,1,0]" : "=v"(r) : "v"(x), "v"(y), "v"(t));
    return r;
}
template <int N> DI void cmul_n(f32x2 (&o)[N], const f32x2 (&x)[N], const f32x2 (&y)[N]) {
    f32x2 t[N];
#pragma unroll
    for (int i = 0; i < N; ++i) t[i] = cm_a(x[i], y[i]);
    SB();
#pragma unroll
    for (int i = 0; i < N; ++i) o[i] = cm_b(x[i], y[i], t[i]);
    SB();
}
DI f32x4 add4(f32x4 a, f32x4 b) {
    f32x4 r;
#pragma unroll
    for (int i = 0; i < 4; ++i) { float t; asm("v_add_f32 %0, %1, %2" : "=v"(t) : "v"(a[i]), "v"(b[i])); r[i] = t; }
    return r;
}
DI f32x4 as_f4(u32x4 v) { return __builtin_bit_cast(f32x4, v); }
DI u32x4 as_u4(f32x4 v) { return __builtin_bit_cast(u32x4, v); }
DI LAS char* lds_at(u32 a) { return (LAS char*)(size_t)a; }
DI u32 opaque(u32 v) { asm volatile("" : "+v"(v)); return v; }
DI f32x2 cexp_step(float a2, float b2, float t) {
    float mag = __builtin_amdgcn_exp2f(a2 * t), ph = __builtin_amdgcn_fractf(b2 * t);
    f32x2 r = {__builtin_amdgcn_cosf(ph), __builtin_amdgcn_sinf(ph)};
    return r * mag;
}
template <class T> DI T lds_ld(LAS char* p) { return *(LAS T*)p; }
template <class T> DI void lds_st(LAS char* p, T v) { *(LAS T*)p = v; }
DI u32x2 lds_tr(LAS char* p) { return __builtin_bit_cast(u32x2, __builtin_amdgcn_ds_read_tr16_b64_v4bf16((LAS bf16x4*)p)); }
DI u32x4 lds_tr2(LAS char* p, int step) { u32x2 a = lds_tr(p), b = lds_tr(p + step); return u32x4{a.x, a.y, b.x, b.y}; }
DI u32x4 make_srd(const void* p, u32 bytes) { u64 a = (u64)p; return u32x4{(u32)a, (u32)(a >> 32) & 0xffffu, bytes, 0x00020000u}; }
DI void dma2(u32& l, u32& so, u32 step, u32 v0, u32 v1, u32x4 srd) {
    asm volatile("s_mov_b32 m0, %0\n\ts_add_u32 %0, %0, 0x800\n\tbuffer_load_dwordx4 %3, %5, %1 offen lds\n\t"
                 "s_add_u32 m0, m0, 0x400\n\ts_add_u32 %1, %1, %2\n\tbuffer_load_dwordx4 %4, %5, %1 offen lds\n\ts_add_u32 %1, %1, %2"
                 : "+s"(l), "+s"(so) : "s"(step), "v"(v0), "v"(v1), "s"(srd) : "memory", "scc");
}
DI void dma3(u32& l, u32& so, u32 step, u32 v0, u32 v1, u32 v2, u32x4 srd) {
    asm volatile("s_mov_b32 m0, %0\n\ts_add_u32 %0, %0, 0xc00\n\tbuffer_load_dwordx4 %3, %6, %1 offen lds\n\t"
                 "s_add_u32 m0, m0, 0x400\n\ts_nop 0\n\tbuffer_load_dwordx4 %4, %6, %1 offen lds\n\t"
                 "s_add_u32 m0, m0, 0x400\n\ts_nop 0\n\tbuffer_load_dwordx4 %5, %6, %1 offen lds\n\ts_add_u32 %1, %1, %2"
                 : "+s"(l), "+s"(so) : "s"(step), "v"(v0), "v"(v1), "v"(v2), "s"(srd) : "memory", "scc");
}
DI u32x4 ws_ld(rsrc_t r, u32 voff, u32 soff = 0) { return __builtin_amdgcn_raw_buffer_load_b128(r, voff, soff, SC1); }
DI void ws_st(rsrc_t r, u32 voff, u32x4 v) { __builtin_amdgcn_raw_buffer_store_b128(v, r, voff, 0, SC1); }
DI u64 ld_flag(char* ws, u32 off) { return __hip_atomic_load((GAS u64*)(ws + off), __ATOMIC_RELAXED, __HIP_MEMORY_SCOPE_AGENT); }
DI void st_flag(char* ws, u32 off, u64 v) { __hip_atomic_store((GAS u64*)(ws + off), v, __ATOMIC_RELAXED, __HIP_MEMORY_SCOPE_AGENT); }
template <int N, int NAP> DI void wait_flags(char* ws, u32 off, u32 stride, u64 tag) {
    for (u32 spin = 0; spin < SPIN_CAP; ++spin) {
        bool ok = true;
#pragma unroll
        for (int i = 0; i < N; ++i) ok &= ld_flag(ws, off + i * stride) == tag;
        if (__all(ok)) break;
        __builtin_amdgcn_s_sleep(NAP);
    }
    __builtin_amdgcn_fence(__ATOMIC_ACQUIRE, "wavefront");
    asm volatile("" ::: "memory");
}
DI void signal_last(LAS char* cnt, u32 n, char* ws, u32 off, u64 tag, int lane) {
    DRAIN();
    u32 old = 0;
    if (lane == 0) old = __hip_atomic_fetch_add((LAS u32*)cnt, 1u, __ATOMIC_RELAXED, __HIP_MEMORY_SCOPE_WORKGROUP);
    old = __builtin_amdgcn_readfirstlane(old);
    if (old == n - 1 && lane == 0) st_flag(ws, off, tag);
}
#define CF() asm volatile("" ::: "memory")
DI void cnt_add(LAS char* c, u32 v = 1) { CF(); __hip_atomic_fetch_add((LAS u32*)c, v, __ATOMIC_RELAXED, __HIP_MEMORY_SCOPE_WORKGROUP); CF(); }
DI u32 cnt_ld(LAS char* c) { CF(); const u32 v = __hip_atomic_load((LAS u32*)c, __ATOMIC_RELAXED, __HIP_MEMORY_SCOPE_WORKGROUP); CF(); return v; }
template <int NAP = 1> DI void cnt_wait(u32 seen, LAS char* c, u32 target) {
    if (__builtin_expect(__any(seen < target), 0)) {
        for (u32 spin = 0; spin < (SPIN_CAP << 5); ++spin) {
            if (!__any(cnt_ld(c) < target)) break;
            __builtin_amdgcn_s_sleep(NAP);
        }
    }
    CF();
}
DI void word_add(LAS char* c, int lane) {
    __builtin_amdgcn_s_waitcnt(0xC07F);
    if (lane == 0) cnt_add(c);
}
template <int CTRL> DI float dpp_add(float v) {
    int t = __builtin_amdgcn_update_dpp(0, __builtin_bit_cast(int, v), CTRL, 0xf, 0xf, true);
    return v + __builtin_bit_cast(float, t);
}
DI float reduce2(float a, float b) {
    auto r = __builtin_amdgcn_permlane32_swap(as_u(a), as_u(b), false, false);
    float t = as_f(r[0]) + as_f(r[1]);
    auto q = __builtin_amdgcn_permlane16_swap(as_u(t), as_u(t), false, false);
    t = as_f(q[0]) + as_f(q[1]);
    t = dpp_add<0x128>(t); t = dpp_add<0x124>(t); t = dpp_add<0x122>(t);
    return dpp_add<0x121>(t);
}
DI float lane_bcast(float v, int l) { return as_f(__builtin_amdgcn_readlane(as_u(v), l)); }

DI void lag_slots(u32 c, u32& base, u32& steps) {
    const u32 e = (c >= 32) + (c >= 64) + (c >= 128);
    base = (128 * e + (8 * c >> e)) * 4;
    steps = e == 0 ? 0x87654321u : e == 1 ? 0x44322210u : e == 2 ? 0x22211100u : 0x11110000u | (c & 1) << 12;
}
DI void get_lags(float (&v)[9], LAS char* src, u32 base, u32 steps) {
#pragma unroll
    for (int i = 0; i < 9; ++i) v[i] = lds_ld<float>(src + base + (i ? (steps >> (4 * i - 4) & 15) * 4 : 0));
}
DI void put_images(LAS char* img, u32 chunk, const float (&v)[9], float first) {
    lds_st<u32x4>(img + 32 + chunk * 16, u32x4{pk_bf(v[0], v[1]), pk_bf(v[2], v[3]), pk_bf(v[4], v[5]), pk_bf(v[6], v[7])});
    lds_st<u32x4>(img + K_SZ + 32 + chunk * 16, u32x4{pk_bf(v[1], v[2]), pk_bf(v[3], v[4]), pk_bf(v[5], v[6]), pk_bf(v[7], v[8])});
    if (chunk < 4) lds_st<u32x4>(img + (chunk >> 1) * K_SZ + (chunk & 1) * 16, u32x4{0, 0, 0, chunk == 3 ? pk_bf(0.f, first) : 0u});
}

template <int NT> struct ConvPlan;
template <> struct ConvPlan<8> {
    static constexpr int k[32] = {-26, -25, -21, -20, -15, -14, -8, 1, 2, 3, 8, 9, 15, 16, 21, 22, 26, 27, 30, 31, 33, 33, 35, 36, 37, 38, 39, 40, 41, 42, 44, 45};
    static constexpr int u1[8] = {10, 17, 23, 28, 32, 34, 43, 48}, u2[8] = {65, 66, 67, 68, 69, 70, 71, 72};
};
template <> struct ConvPlan<6> {
    static constexpr int k[24] = {-18, -17, -15, -14, -11, -10, -6, 1, 2, 3, 6, 7, 11, 12, 15, 16, 18, 18, 20, 21, 22, 23, 29, 30};
    static constexpr int u1[6] = {8, 13, 17, 19, 26, 33}, u2[6] = {37, 38, 39, 40, 41, 42};
};

template <bool MAIN>
DI void gate_up(LAS char* smem, rsrc_t wsr, int w, int lane, int tok0, int f0 DBGPARS) {
    constexpr int NT = MAIN ? 6 : 4, NG = MAIN ? 2 : 3, NW = 2 * NG;
    constexpr u32 GOFF = NT * 4096, WROW = NG * 64, UREL = 64 * WROW;
    const int mi = w & 1, ni = w >> 1, c16 = lane & 15, g4 = lane >> 4, q4 = c16 >> 2, p4 = lane & 3;
    LAS char* const cR = smem + L_CR + lane * 4;
    LAS char* const cD = smem + L_CD + lane * 4;
    f32x4 acc[NW][NT];
    u32 ax[2], aw[NG];
#pragma unroll
    for (int s = 0; s < 2; ++s) ax[s] = (16 * NT * ni + c16) * 128 + (((4 * s + g4) ^ (c16 >> 1)) << 4);
#pragma unroll
    for (int m = 0; m < NG; ++m) aw[m] = GOFF + (8 * g4 + q4) * WROW + (((NG * mi + m) ^ (MAIN ? (q4 >> 1) | ((g4 & 1) << 1) : g4 & 1)) << 5) + p4 * 8;
    u32x4 fw[2][NW], fx[2][NT];
    const u32 mine = g4 == w;
    u32 slot = 0, need = 0;
    auto rdw = [&](int set, int s, int m) INL { fw[set][m] = lds_tr2(smem + slot + aw[m >> 1] + (m & 1) * UREL + s * 32 * WROW, 4 * WROW); };
    auto rdx = [&](int set, int s, int n) INL { fx[set][n] = lds_ld<u32x4>(smem + slot + ax[s] + n * 2048); };
    auto group = [&](int set, int s, bool first, bool read, bool sync) INL {
        u32 seen = 0;
#pragma unroll
        for (int i = 0; i < 24; ++i) {
            acc[i / NT][i % NT] = mma16(fw[set][i / NT], fx[set][i % NT], first ? f32x4{0.f, 0.f, 0.f, 0.f} : acc[i / NT][i % NT]);
            SB();
            if (read) {
                if (i == 0) rdx(set ^ 1, s, 0);
                if (i == 1) rdw(set ^ 1, s, 0);
                if (i >= 3 && i < 2 + NT) rdx(set ^ 1, s, i - 2);
                if (i >= 2 + NT && i < NT + 2 * NW && ((i - NT) & 1) == 0) rdw(set ^ 1, s, (i - NT) >> 1);
            }
            if (sync) {
                if (i == 16) seen = cnt_ld(cR);
                if (i == 18) slot = slot == 2 * SLOT ? 0 : slot + SLOT;
                if (i == 21) {
                    __builtin_amdgcn_s_waitcnt(0xC07F);
                    cnt_add(cD, mine);
                    cnt_wait(seen, cR, need);
                    need = min(need + 1, 8u);
                }
            }
            SB();
        }
    };
    rdx(0, 0, 0); rdw(0, 0, 0);
#pragma unroll
    for (int n = 1; n < NT; ++n) rdx(0, 0, n);
#pragma unroll
    for (int m = 1; m < NW; ++m) rdw(0, 0, m);
    SB();
    group(0, 1, true, true, true);
#pragma unroll 1
    for (int t = 1; t < 10; ++t) {
        group(1, 0, false, true, false);
        group(0, 1, false, true, true);
    }
    group(1, 0, false, false, false);
    STAMP(12);
    cnt_wait(0, smem + M_SD, 2);
    f32x4 cs[NW];
#pragma unroll
    for (int m = 0; m < NW; ++m) cs[m] = lds_ld<f32x4>(smem + L_CS + ((m & 1) * 32 * NG + 16 * (NG * mi + (m >> 1)) + 4 * g4) * 4);
    const int grp0 = (f0 >> 4) + NG * mi;
    const u32 hoff = WS_H + (((tok0 >> 4) + NT * ni) * 48 * 64 + lane) * 16;
#pragma unroll
    for (int n = 0; n < NT; ++n) {
        const u32 st = lds_ld<u32>(smem + L_STAT + (16 * (NT * ni + n) + c16) * 4);
        const float nm = -bf_lo(st), sd = bf_hi(st), k1 = -1.44269504089f * __builtin_amdgcn_rcpf(sd), k2 = sd * sd;
        u32 hv[2 * NG];
#pragma unroll
        for (int e = 0; e < 2 * NG; ++e) {
            const int m = e & ~1, r = 2 * (e & 1);
            const f32x2 g = f32x2{cs[m][r], cs[m][r + 1]} * nm + f32x2{acc[m][n][r], acc[m][n][r + 1]};
            const f32x2 u = f32x2{cs[m + 1][r], cs[m + 1][r + 1]} * nm + f32x2{acc[m + 1][n][r], acc[m + 1][n][r + 1]};
            const f32x2 t = g * k1;
            const f32x2 ex = {__builtin_amdgcn_exp2f(t.x), __builtin_amdgcn_exp2f(t.y)};
            const f32x2 d = ex * k2 + k2;
            const f32x2 rc = {__builtin_amdgcn_rcpf(d.x), __builtin_amdgcn_rcpf(d.y)};
            hv[e] = pk_bf(g * u * rc);
        }
        const u32 o = hoff + n * 48 * 1024;
        if (MAIN) {
            ws_st(wsr, o + (grp0 >> 1) * 1024, u32x4{hv[0], hv[1], hv[2], hv[3]});
        } else {
            const u32x4 full = mi ? u32x4{hv[2], hv[3], hv[2 * NG - 2], hv[2 * NG - 1]} : u32x4{hv[0], hv[1], hv[2], hv[3]};
            const u32x2 half = mi ? u32x2{hv[0], hv[1]} : u32x2{hv[2 * NG - 2], hv[2 * NG - 1]};
            ws_st(wsr, o + ((grp0 + 1) >> 1) * 1024, full);
            __builtin_amdgcn_raw_buffer_store_b64(half, wsr, o + ((f0 >> 5) + 1) * 1024 + mi * 8, 0, SC1);
        }
        if (n == EPN * NT / 6) word_add(smem + M_EP, lane);
        SB();
    }
}

DI int kperm(int k, int ctile) { return (ctile & 4) ? (k + 3) % NK : k; }
template <bool LOADY, class F>
DI void phase2(LAS char* smem, char* ws, rsrc_t wsr, const u16* x, u16* out, u32x4 (&fb)[NK][5], int w, int lane, int ttile,
               int ctile, F&& extra, u64 tag DBGPARS) {
    const int ks0 = NK * w, c16 = lane & 15, g4 = lane >> 4;
    constexpr int NOUT = LOADY ? 3 : 2;
    u32x2 xv[NOUT];
    u32 oo[NOUT], pr[NOUT];
    u32x4 sel;
#pragma unroll
    for (int i = 0; i < NOUT; ++i) {
        const int tl = w + 8 * i, tm = (tl * 13) >> 6, tn = tl - 5 * tm;
        oo[i] = opaque(((64 * ttile + 16 * tm + c16) * 640 + 80 * ctile + 16 * tn + 4 * g4) * 2);
        pr[i] = opaque((u32)(size_t)smem + (16 * tn + c16) * PROW + (((2 * tm + (g4 & 1)) ^ (c16 >> 1)) << 4) + (g4 >> 1) * PWAVE);
    }
#pragma unroll
    for (int e = 0; e < 4; ++e) {
        const int dd = c16 - 8 * (g4 & 1) - 2 * e;
        sel[e] = opaque(dd == 0 ? 0x3f80u : dd == 1 ? 0x3f800000u : 0u);
    }
    const u32 pw = opaque((u32)(size_t)smem + w * PWAVE + c16 * PROW + (((g4 >> 1) ^ (c16 >> 1)) << 4) + (g4 & 1) * 8);
    STAMP(15);
    if (LOADY) {
        const int wk = w + (lane & 4), i3 = min(lane & 3, 2);
        const u32 fo = WS_F2 + (ttile < 30 ? ((ttile * 43) >> 7) * 24 + 3 * wk + i3 : 240 + 2 * wk + (i3 & 1)) * 128;
        const u64 peek = ld_flag(ws, fo);
        extra();
        if (!__all(peek == tag)) wait_flags<1, NAP2>(ws, fo, 0, tag);
        __builtin_amdgcn_fence(__ATOMIC_ACQUIRE, "wavefront");
        CF();
        word_add(smem + M_F2 + w * 4, lane);
    } else {
        extra();
        cnt_wait(0, smem + M_F2 + (w - 4) * 4, 1);
    }
    STAMP(16);
    u32x4 fh[4][NK];
    auto ldh = [&](int m, int k) INL {
        fh[m][k] = ws_ld(wsr, WS_H + lane * 16 + (k % 3) * 1024, ((4 * ttile + ((m + ctile) & 3)) * 48 + ks0 + kperm(k - k % 3, ctile)) * 1024);
    };
    constexpr int NFIRST = LOADY ? AHEAD : 4 * NK;
#pragma unroll
    for (int i = 0; i < NFIRST; ++i) {
        if (!LOADY && i == AHEAD) cnt_wait(0, smem + M_YS, 3);
        ldh(i / NK, i % NK);
        SB();
    }
    STAMP(22);
    cnt_wait(0, smem + M_C2, 4);
    f32x4 d[2][5];
    u32x2 pd[5];
#pragma unroll
    for (int m = 0; m < 5; ++m) {
#pragma unroll
        for (int i = 0; i < (m < 4 ? 5 * NK : 12); ++i) {
            if (m < 4) {
                d[m & 1][i % 5] = mma16(fh[m][i / 5], fb[i / 5][i % 5], i < 5 ? f32x4{0.f, 0.f, 0.f, 0.f} : d[m & 1][i % 5]);
                SB();
            }
            if (m > 0 && i >= 2 && i < 12) {
                const int j = (i - 2) >> 1;
                const f32x4 v = d[(m - 1) & 1][j];
                if ((i & 1) == 0) pd[j].x = pk_bf(v[0], v[1]);
                else { pd[j].y = pk_bf(v[2], v[3]); lds_st<u32x2>(lds_at(pw ^ (((m - 1 + ctile) & 3) << 5)) + j * 16 * PROW, pd[j]); }
            }
            if (LOADY && m == 0 && i == 5 * NK - 1) {
                cnt_wait(0, smem + M_YS, 3);
#pragma unroll
                for (int e = NFIRST; e < 4 * NK; ++e) { ldh(e / NK, e % NK); SB(); }
            }
            if (m == 1 && i == 0) STAMPB(6);
            if (m == 3 && i == 0) STAMPB(7);
            if (m == 2 && i == 17) {
#pragma unroll
                for (int e = 0; e < NOUT; ++e) xv[e] = *(const u32x2*)((const char*)x + oo[e]);
            }
            SB();
        }
    }
    STAMP(17);
    if (LOADY) {
        if (w == 0) {
            wait_flags<1, NAP3>(ws, WS_F3 + (32 * ctile + (lane & 31)) * 8, 0, tag);
            word_add(smem + M_F3, lane);
        }
        cnt_wait(0, smem + M_F3, 1);
        u32x4 yv[3];
#pragma unroll
        for (int i = 0; i < 3; ++i) {
            const u32 n = min((4 * i + w) * 64 + lane, 639);
            yv[i] = ws_ld(wsr, WS_YT + (80 * ctile + (n >> 3)) * 4096 + 128 * ttile + (n & 7) * 16);
        }
#pragma unroll
        for (int i = 0; i < 3; ++i) {
            const u32 n = min((4 * i + w) * 64 + lane, 639);
            lds_st<u32x4>(smem + L_Y + (n >> 3) * PROW + (((n ^ (n >> 4)) & 7) << 4), yv[i]);
        }
    }
    u32x4 sely;
#pragma unroll
    for (int e = 0; e < 4; ++e) sely[e] = opaque(g4 < 2 ? sel[e] : 0u);
    STAMP(18);
    BAR();
    STAMP(19);
    auto finish = [&](auto nt) INL {
        constexpr int N = decltype(nt)::value;
        u32x4 pf[N][5];
#pragma unroll
        for (int i = 0; i < N; ++i) {
#pragma unroll
            for (int s = 0; s < 4; ++s) pf[i][s] = lds_ld<u32x4>(lds_at(pr[i]) + 2 * s * PWAVE);
            pf[i][4] = lds_ld<u32x4>(lds_at(pr[i] + L_Y - (g4 >> 1) * PWAVE));
        }
        SB();
#pragma unroll
        for (int i = 0; i < N; ++i) {
            f32x4 f = {0.f, 0.f, 0.f, 0.f};
#pragma unroll
            for (int s = 0; s < 4; ++s) f = mma16(pf[i][s], sel, f);
            const f32x4 y = mma16(pf[i][4], sely, f32x4{0.f, 0.f, 0.f, 0.f});
            const u32 a0 = pk_bf(bf_lo(xv[i].x) + y[0], bf_hi(xv[i].x) + y[1]), a1 = pk_bf(bf_lo(xv[i].y) + y[2], bf_hi(xv[i].y) + y[3]);
            const u32 f0 = pk_bf(f[0], f[1]), f1 = pk_bf(f[2], f[3]);
            *(u32x2*)((char*)out + oo[i]) = u32x2{pk_bf(bf_lo(a0) + bf_lo(f0), bf_hi(a0) + bf_hi(f0)), pk_bf(bf_lo(a1) + bf_lo(f1), bf_hi(a1) + bf_hi(f1))};
        }
    };
    finish(std::integral_constant<int, NOUT>{});
    STAMPB(9);
}
#endif

__global__ void __launch_bounds__(512, 2)
fused_kernel(const u16* __restrict__ x, const u16* __restrict__ Are, const u16* __restrict__ Aim, const u16* __restrict__ Bre,
             const u16* __restrict__ Bim, const u16* __restrict__ Cre, const u16* __restrict__ Cim, const u16* __restrict__ logdt,
             const u16* __restrict__ Dskip, const u16* __restrict__ Wg, const u16* __restrict__ Wu, const u16* __restrict__ Wd,
             u16* __restrict__ out, char* ws) {
#if defined(__HIP_DEVICE_COMPILE__)
    extern __shared__ __attribute__((aligned(16))) char smem_[];
    LAS char* const smem = (LAS char*)smem_;
    const u32 lds0 = (u32)(size_t)smem;
    const int b = blockIdx.x, xcd = b & 7, wi = b >> 3;
    const int w = __builtin_amdgcn_readfirstlane(threadIdx.x >> 6), lane = threadIdx.x & 63, w4 = w & 3;
    {
        u64 pc;
        asm volatile("s_getpc_b64 %0" : "=s"(pc));
        const u32 o = (wi * (CODE_LINES / 32) + min((u32)threadIdx.x, CODE_LINES / 32 - 1)) * 128;
        asm volatile("s_mov_b32 m0, %0\n\ts_nop 0\n\tglobal_load_lds_dword %1, %2" ::"s"(lds0 + L_SINK), "v"(o), "s"(pc) : "memory");
    }
    asm volatile("" ::"s"(x), "s"(Are), "s"(Aim), "s"(Bre), "s"(Bim), "s"(Cre), "s"(Cim), "s"(logdt), "s"(Dskip), "s"(Wg), "s"(Wu), "s"(Wd), "s"(out), "s"(ws));
    if (threadIdx.x < 38) lds_st<u32x4>(smem + L_MISC + threadIdx.x * 16, u32x4{0, 0, 0, 0});
    const rsrc_t wsr = __builtin_amdgcn_make_buffer_rsrc(ws, 0, WS_END, 0x00020000);
    const bool mainwg = wi < 30;
    const int fq = (wi * 13) >> 6, tt = 5 * (xcd & 1) + wi - 5 * fq, fl = 6 * (xcd >> 1) + fq, sl = 2 * xcd + wi - 30;
    const int tok0 = mainwg ? 192 * tt : 1920, f0 = mainwg ? 64 * fl : 96 * sl;
    const int sb = 32 * xcd + wi, odd = sb & 1, ch0 = (5 * sb + 1) >> 1;
    const int ttile = 4 * xcd + (wi & 3), ctile = wi >> 2;
    STAMP(0);

    const int chs = odd ? ch0 - 1 : ch0 + 2;
    const int w3 = (w * 11) >> 5;
    const int kch = w3 == 2 ? chs : ch0 + w3;
    const int mode0 = (w - 3 * w3) * (w3 == 2 ? 48 : 64) + (w3 == 2 ? 96 * odd : 0);
    const bool mvalid = w < 6 || lane < 48;
    u32 ldt, dsk;
    u64 tag;
    asm volatile("s_load_dword %0, %3, %6\n\ts_load_dword %1, %4, %6\n\ts_load_dwordx2 %2, %5, 0x0"
                 : "=&s"(ldt), "=&s"(dsk), "=&s"(tag) : "s"(logdt), "s"(Dskip), "s"(ws), "s"((kch >> 1) * 4) : "memory");
    const int lb = 128 * (xcd & 1) + 32 * (xcd >> 1) + wi;
    const int wks = b / 5, wnb = b % 5;
    const int g4 = lane >> 4, q4 = (lane & 15) >> 2, p4 = lane & 3;
    u32 prm[6], xr[2][5];
    u32x4 wd[2];

    auto load_params = [&]() INL {
        const u32 o = (kch * 192 + mode0 + (mvalid ? lane : 0)) * 2;
        asm volatile("global_load_ushort %0, %6, %7\n\tglobal_load_ushort %1, %6, %8\n\tglobal_load_ushort %2, %6, %9\n\t"
                     "global_load_ushort %3, %6, %10\n\tglobal_load_ushort %4, %6, %11\n\tglobal_load_ushort %5, %6, %12"
                     : "=&v"(prm[0]), "=&v"(prm[1]), "=&v"(prm[2]), "=&v"(prm[3]), "=&v"(prm[4]), "=&v"(prm[5])
                     : "v"(o), "s"(Are), "s"(Aim), "s"(Bre), "s"(Bim), "s"(Cre), "s"(Cim) : "memory");
    };
    auto load_rows = [&]() INL {
        const u32 o = (8 * lb + 2 * w4) * 1280 + lane * 4;
        asm volatile("global_load_dword %0, %10, %11\n\tglobal_load_dword %1, %10, %11 offset:256\n\tglobal_load_dword %2, %10, %11 offset:512\n\t"
                     "global_load_dword %3, %10, %11 offset:768\n\tglobal_load_dword %4, %10, %11 offset:1024\n\t"
                     "global_load_dword %5, %10, %11 offset:1280\n\tglobal_load_dword %6, %10, %11 offset:1536\n\tglobal_load_dword %7, %10, %11 offset:1792\n\t"
                     "global_load_dword %8, %10, %11 offset:2048\n\tglobal_load_dword %9, %10, %11 offset:2304"
                     : "=&v"(xr[0][0]), "=&v"(xr[0][1]), "=&v"(xr[0][2]), "=&v"(xr[0][3]), "=&v"(xr[0][4]), "=&v"(xr[1][0]), "=&v"(xr[1][1]),
                       "=&v"(xr[1][2]), "=&v"(xr[1][3]), "=&v"(xr[1][4])
                     : "v"(o), "s"(x) : "memory");
    };
    auto load_wd = [&]() INL {
        const u32 od = (32 * min(wks, 47) + (lane >> 2)) * 1280 + wnb * 256 + w4 * 64 + p4 * 16;
        asm volatile("global_load_dwordx4 %0, %2, %4\n\tglobal_load_dwordx4 %1, %3, %4" : "=&v"(wd[0]), "=&v"(wd[1]) : "v"(od), "v"(od + 16 * 1280), "s"(Wd) : "memory");
    };
    if (w >= 4) { load_rows(); load_wd(); }
    load_params();
    const u32x4 xs = make_srd(x, 2048 * 1280), gs = make_srd(Wg, 640 * 3072), us = make_srd(Wu, 640 * 3072);
    u32 vx[2], vw[3];
#pragma unroll
    for (int p = 0; p < 2; ++p) vx[p] = (lane >> 3) * 1280 + (((lane & 7) ^ (4 * p + g4)) << 4);
#pragma unroll
    for (int j = 0; j < 3; ++j) {
        const u32 n = 64 * j + lane, row = n / 12;
        vw[j] = mainwg ? (lane >> 3) * 3072 + (((lane & 7) ^ (2 * (g4 & 1) + 4 * (j & 1))) << 4) : row * 3072 + (((n % 12) ^ ((row >> 3) << 1)) << 4);
    }
    const u32 rot = mainwg ? tt : sl & 7;
    u32 lx, sx, lw, sw;
    auto aim = [&](u32 n, u32 p, u32 slot, u32 kt) INL {
        const u32 nx = (mainwg ? 24 : 16) / n;
        lx = lds0 + slot + p * nx * 1024;
        sx = tok0 * 1280 + kt * 128 + p * nx * 10240;
        lw = lds0 + slot + (mainwg ? 24576 : 16384) + p * ((mainwg ? 8192 : 12288) / n);
        sw = (kt * 64 + p * (64 / n)) * 3072 + f0 * 2;
    };
    auto xpairs = [&](int p0, int p1) INL {
        u32 l = lx + p0 * 2048, so = sx + p0 * 20480;
#pragma unroll
        for (int i = p0; i < p1; ++i) dma2(l, so, 10240, vx[0], vx[1], xs);
    };
    auto wrows = [&](int n16, int which) INL {
        if (mainwg) {
#pragma unroll
            for (int up = 0; up < 2; ++up) {
                u32 l = lw + up * 8192, so = sw;
#pragma unroll
                for (int i = 0; i < n16; ++i) if (which >> up & 1) dma2(l, so, 8 * 3072, vw[0], vw[1], up ? us : gs);
            }
        } else {
#pragma unroll
            for (int up = 0; up < 2; ++up) {
                u32 l = lw + up * 12288, so = sw;
#pragma unroll
                for (int i = 0; i < n16; ++i) if (which >> up & 1) dma3(l, so, 16 * 3072, vw[0], vw[1], vw[2], up ? us : gs);
            }
        }
    };
    LAS char* const cR = smem + L_CR + lane * 4;
    LAS char* const cD = smem + L_CD + lane * 4;
    u32 lkt = 2 - (2 <= rot), lslot = 2 * SLOT;
    auto stage_a = [&](bool sig) INL {
        aim(2, w & 1, lslot, lkt);
        xpairs(0, 2);
        if (sig) { asm volatile("s_waitcnt vmcnt(4)" ::: "memory"); cnt_add(cR, (lane >> 5) == (w & 1)); }
        xpairs(2, 4);
        if (mainwg) xpairs(4, 6);
    };
    auto stage_b = [&]() INL {
        aim(2, w & 1, lslot, lkt);
        wrows(2, 3);
        lslot = lslot == 2 * SLOT ? 0 : lslot + SLOT;
        lkt += lkt + 1 == rot ? 2 : 1;
    };
    STAMPA(4);
    BAR();
    STAMPA(5);
    asm volatile("" : "+s"(ldt), "+s"(dsk), "+s"(tag)::"memory");
    if (w >= 4) __builtin_amdgcn_s_setprio(1);
    aim(4, w4, w < 4 ? 0 : SLOT, w < 4 ? rot : rot == 0);
    STAMPA(6);
    tag += 1;
    float dt = rnd_bf(expf(bf_lo(ldt >> (16 * (kch & 1)))));
    const u32 tab = lds0 + L_TAB + w * TAB_SZ;
    u32 wa = tab + lane * 128 + ((((lane & 2) << 1) | ((lane & 4) >> 1) | (lane & 1)) << 4);
    u32 ra = tab + (8 * (lane >> 5) + q4) * 128 + (((2 * (g4 & 1) + (p4 >> 1)) ^ (((q4 & 2) << 1) | (q4 & 1))) << 4) + (p4 & 1) * 8;
    const u32 ptw = lds0 + (w >= 5 ? L_TAB + w * TAB_SZ : L_TOP + w * PT_SZ);
    u32 pa[4] = {ptw + (8 * (lane & 31) + 4 * (lane >> 5)) * 4, ptw + (lane < 32 ? 256 + 4 * lane : 320 + 2 * lane) * 4,
                 ptw + (lane < 32 ? 258 + 4 * lane : 384 + 2 * lane) * 4, ptw + (lane < 32 ? 512 + lane : 640) * 4};
    float skip = (lane == 0 && mode0 == 0) ? bf_lo(dsk >> (16 * (kch & 1))) : 0.f;
    const u32 chunk = 64 * w4 + lane;
    u32 gbase, gsteps;
    lag_slots(chunk, gbase, gsteps);
    const u32 sn = min(threadIdx.x, 482u), c3 = (sn >= 161) + (sn >= 322), so = lds0 + (sn - 161 * c3) * 16;
    u32 sa[4] = {so + (c3 == 2 ? L_PT5 + TAB_SZ : L_TOP + 3 * c3 * PT_SZ), so + (c3 == 2 ? L_PT5 + 2 * TAB_SZ : L_TOP + (3 * c3 + 1) * PT_SZ),
                 so + (c3 == 0 ? L_TOP + 2 * PT_SZ : L_PT5), so + (c3 == 2 ? L_KS : L_TOP + 3 * c3 * PT_SZ)};
    asm volatile("" : "+v"(gbase), "+v"(gsteps), "+v"(sa[0]), "+v"(sa[1]), "+v"(sa[2]), "+v"(sa[3]));
    asm volatile("" : "+v"(dt), "+v"(wa), "+v"(ra), "+v"(pa[0]), "+v"(pa[1]), "+v"(pa[2]), "+v"(pa[3]), "+v"(skip), "+v"(vw[2]));
    DRAIN();
    asm volatile("" : "+v"(prm[0]), "+v"(prm[1]), "+v"(prm[2]), "+v"(prm[3]), "+v"(prm[4]), "+v"(prm[5])::"memory");
    STAMP(1);
    u32x4 pub[5];
    u32 stat = 0;
    if (w >= 4) {
        asm volatile("" : "+v"(xr[0][0]), "+v"(xr[0][1]), "+v"(xr[0][2]), "+v"(xr[0][3]), "+v"(xr[0][4]), "+v"(xr[1][0]), "+v"(xr[1][1]),
                          "+v"(xr[1][2]), "+v"(xr[1][3]), "+v"(xr[1][4]), "+v"(wd[0]), "+v"(wd[1])::"memory");
        LAS char* const stg = lds_at(tab);
#pragma unroll
        for (int i = 0; i < 2; ++i) lds_st<u32x4>(stg + i * 1024 + lane * 16, wd[i]);
        float sm[2] = {0.f, 0.f}, sq[2] = {0.f, 0.f};
#pragma unroll
        for (int q = 0; q < 5; ++q)
#pragma unroll
            for (int r = 0; r < 2; ++r) {
                sm[r] = __builtin_amdgcn_fdot2_f32_bf16(as_bf2(xr[r][q]), as_bf2(0x3f803f80u), sm[r], false);
                sq[r] = __builtin_amdgcn_fdot2_f32_bf16(as_bf2(xr[r][q]), as_bf2(xr[r][q]), sq[r], false);
            }
        const float mu = reduce2(sm[0], sm[1]) * (1.f / 640.f), m2 = reduce2(sq[0], sq[1]) * (1.f / 640.f);
        const float mb = rnd_bf(mu), sd = rnd_bf(__builtin_amdgcn_sqrtf(rnd_bf(rnd_bf(fmaxf(fmaf(-mu, mu, m2), 0.f)) + 1e-5f)));
        const float rs = __builtin_amdgcn_rcpf(sd), mrs = -mb * rs;
        stat = pk_bf(mb, sd);
        u32 o[2][5];
#pragma unroll
        for (int r = 0; r < 2; ++r) {
            const float k = lane_bcast(rs, 32 * r), m = lane_bcast(mrs, 32 * r);
#pragma unroll
            for (int q = 0; q < 5; ++q) o[r][q] = pk_bf(f32x2{bf_lo(xr[r][q]), bf_hi(xr[r][q])} * k + m);
        }
        SB();
#pragma unroll
        for (int r = 0; r < 2; ++r)
#pragma unroll
            for (int q = 0; q < 5; ++q) lds_st<u32>(smem + L_LN + (7 - 2 * w4 - r) * LNROW + q * 256 + lane * 4, o[r][q]);
        word_add(smem + M_LN, lane);
#pragma unroll
        for (int c = 0; c < 2; ++c) pub[c] = lds_tr2(stg + (4 * g4 + q4) * 64 + p4 * 8 + c * 32, 1024);
        cnt_wait(0, smem + M_LN, 4);
#pragma unroll
        for (int i = 0; i < 3; ++i) pub[2 + i] = lds_tr2(smem + L_LN + q4 * LNROW + min(4 * i + w4, 9) * 128 + g4 * 32 + p4 * 8, 4 * LNROW);
        word_add(smem + M_LR, lane);
        STAMP(20);
    } else {
        xpairs(0, 2);
        wrows(1, 1);
    }
    auto publish = [&](int i) INL {
        if (w < 4) return;
        if (i < 2) {
            const int ct = 8 * wnb + 2 * w4 + i;
            ws_st(wsr, b < 240 ? WS_WDF + lane * 16 + (((ct / 5) * 48 + wks) * 5 + ct % 5) * 1024 : WS_END, pub[i]);
        } else if (4 * (i - 2) + w4 < 10) {
            ws_st(wsr, WS_XNT + (lb * 640 + (4 * (i - 2) + w4) * 64 + lane) * 16, pub[i]);
        }
        if (i == 0) __builtin_amdgcn_raw_buffer_store_b32(stat, wsr, (lane & 31) == 0 ? WS_STAT + (8 * lb + 2 * w4 + (lane >> 5)) * 4 : WS_END, 0, SC1);
    };
    {
        u32x4 vre, vim;
        auto put = [&](int c, const f32x2 (&v)[8]) INL {
            vre = u32x4{pk_bf(v[0].x, v[1].x), pk_bf(v[2].x, v[3].x), pk_bf(v[4].x, v[5].x), pk_bf(v[6].x, v[7].x)};
            vim = u32x4{pk_bf(v[0].y, v[1].y), pk_bf(v[2].y, v[3].y), pk_bf(v[4].y, v[5].y), pk_bf(v[6].y, v[7].y)};
            SB();
            lds_st<u32x4>(lds_at(wa ^ (c << 4)), vre);
            lds_st<u32x4>(lds_at(wa ^ ((c + 4) << 4)), vim);
            SB();
        };
        auto hold = [&]() INL { asm volatile("" ::"v"(vre), "v"(vim)); };
        auto get = [&](int s) INL {
            const u32x2 lo = lds_tr(lds_at(ra ^ ((s & 1) << 6)) + (s >> 1) * 2048), hi = lds_tr(lds_at(ra ^ (((s & 1) << 6) | 32)) + (s >> 1) * 2048 + 512);
            return u32x4{lo.x, lo.y, hi.x, hi.y};
        };
        const float a2 = dt * bf_lo(prm[0]) * 1.44269504089f, b2 = dt * bf_lo(prm[1]) * 0.15915494309f;
        const f32x2 dB = {dt * bf_lo(prm[2]), -dt * bf_lo(prm[3])}, Cc = {bf_lo(prm[4]), -bf_lo(prm[5])};
        f32x2 W = cmul(Cc, dB);
        if (!mvalid) W = f32x2{0.f, 0.f};
        const f32x2 e1 = cexp_step(a2, -b2, 1.f), e256 = cexp_step(a2, -b2, 256.f), e8 = cexp_step(a2, b2, 8.f);
        SB();
        f32x2 l1[5], l2[8], l3[11], l4[5];
        cmul_n<5>(l1, {e1, e8, e256, W, W}, {e1, e8, e256, e1, e256});
        const f32x2 e2 = l1[0], e16 = l1[1], e512 = l1[2];
        cmul_n<8>(l2, {e2, e16, e16, W, l1[3], l1[4], W, l1[4]}, {e2, e16, e8, e2, e2, e2, e512, e512});
        const f32x2 e4 = l2[0], e32 = l2[1];
        STAMPA(7);
        publish(2);
        cmul_n<11>(l3, {W, l1[3], l2[3], l2[4], l1[4], l2[5], l2[6], l2[7], l2[6], l2[7], e32}, {e4, e4, e4, e4, e4, e4, e4, e4, e512, e512, e32});
        const f32x2 e64 = l3[10];
        put(0, {W, l1[3], l2[3], l2[4], l3[0], l3[1], l3[2], l3[3]});
        if (w < 4) wrows(1, 2);
        else xpairs(0, 1);
        cmul_n<5>(l4, {l3[8], l3[9], e8, e16, l2[2]}, {e512, e512, e32, e32, e32});
        hold();
        put(1, {l1[4], l2[5], l3[4], l3[5], l2[6], l3[6], l2[7], l3[7]});
        const f32x2 q4a = cm_a(l4[0], e512);
        publish(3);
        const f32x2 qe = cm_b(l4[0], e512, q4a);
        f32x2 c[8] = {f32x2{1.f, 0.f}, e8, e16, l2[2], e32, l4[2], l4[3], l4[4]}, cn[8];
        const f32x2 m64[8] = {e64, e64, e64, e64, e64, e64, e64, e64};
        cmul_n<8>(cn, c, m64);
        hold();
        put(2, {l3[8], l3[9], l4[0], l4[1], qe, qe, qe, qe});
        STAMPA(8);
        if (w >= 4) xpairs(1, 2);
        if (mainwg) xpairs(2, 3);
        u32x4 fa[8];
#pragma unroll
        for (int s = 0; s < 8; ++s) fa[s] = get(s);
        SB();
        hold();
        put(0, c);
        publish(4);
        cmul_n<8>(c, cn, m64);
        hold();
        put(1, cn);
        if (w >= 4) wrows(1, 1);
        publish(0);
        cmul_n<8>(cn, c, m64);
        hold();
        put(2, c);
        if (w >= 4) wrows(1, 2);
        publish(1);
        hold();
        put(3, cn);
        STAMPA(9);
        f32x16 K = zero16();
#pragma unroll
        for (int s = 0; s < 8; ++s) K = mma32(fa[s], get(s), K);
        cnt_wait(cnt_ld(smem + M_LR), smem + M_LR, 4);
        lds_st<f32x4>(lds_at(pa[0]), f32x4{K[0] + skip, K[1], K[2], K[3]});
        lds_st<f32x2>(lds_at(pa[1]), f32x2{K[4], K[5]});
        lds_st<f32x2>(lds_at(pa[2]), f32x2{K[6], K[7]});
        if (lane <= 32) lds_st<float>(lds_at(pa[3]), K[8]);
        if (lane < 32) {
#pragma unroll
            for (int i = 1; i < 4; ++i) lds_st<float>(lds_at(pa[3]) + 128 * i, K[8 + i]);
        }
    }
    STAMP(2);
    DRAIN();
    __builtin_amdgcn_s_setprio(0);
    BAR();
    STAMPA(11);
    {
        if (w >= 6) stage_a(false);
        const f32x4 v2 = lds_ld<f32x4>(lds_at(sa[2]));
        const f32x4 v = lds_ld<f32x4>(lds_at(sa[0])) + lds_ld<f32x4>(lds_at(sa[1])) + (sa[3] == sa[0] ? v2 : f32x4{0.f, 0.f, 0.f, 0.f});
        lds_st<f32x4>(lds_at(sa[3]), v);
        f32x4 cz = {0.f, 0.f, 0.f, 0.f};
        if ((w == 4 || w == 5) && mainwg) {
            const u32 sw4 = (q4 >> 1) | ((g4 & 1) << 1);
#pragma unroll
            for (int m = 0; m < 8; ++m) {
                const u32 one = (lane & 15) == (m >> 1) ? 0x3f803f80u : 0u;
                cz = mma16(lds_tr2(smem + 24576 + (8 * g4 + q4) * 128 + (((2 * w4 + (m >> 2)) ^ sw4) << 5) + p4 * 8 + (m & 3) * 4096, 512), u32x4{one, one, one, one}, cz);
            }
        }
        if (w >= 6) stage_b();
        BAR();
        if (w == 4 || w == 5) {
            if (w == 4 && lane == 0) st_flag(ws, WS_F1 + b * 8, tag);
            ws_st(wsr, mainwg && (lane & 15) < 4 ? WS_CSP + tt * 12288 + (lane & 1) * 6144 + (f0 + 32 * w4 + 8 * (lane & 2) + 4 * g4) * 4 : WS_END, as_u4(cz));
#pragma unroll
            for (int i = 0; i < 2; ++i) {
                const u32 o = min(128 * i + 64 * w4 + lane, 160) * 16;
                ws_st(wsr, WS_KP + sb * PT_SZ + o, lds_ld<u32x4>(smem + L_KS + o));
            }
        }
        LAS char* const src = smem + L_TOP + (w >> 2) * 3 * PT_SZ;
        float g[9];
        get_lags(g, src, gbase, gsteps);
        put_images(smem + L_K + (w >> 2) * 2 * K_SZ, chunk, g, lds_ld<float>(src));
    }
    STAMP(3);
    u32x4 fb[NK][5];
    auto load_fb = [&](int k0, int k1) INL {
#pragma unroll
        for (int k = k0; k < k1; ++k)
#pragma unroll
            for (int j = 0; j < 5; ++j) fb[k][j] = ws_ld(wsr, WS_WDF + lane * 16 + (j & 3) * 1024, ((ctile * 48 + NK * w + kperm(k, ctile)) * 5 + (j & 4)) * 1024);
    };

    if (w < 4) {
        __builtin_amdgcn_s_setprio(2);
        if (mainwg) gate_up<true>(smem, wsr, w, lane, tok0, f0 DBGARGS);
        else gate_up<false>(smem, wsr, w, lane, tok0, f0 DBGARGS);
        STAMP(13);
        signal_last(smem + M_C2, 4, ws, WS_F2 + (mainwg ? tt * 24 + fl : 240 + sl) * 128, tag, lane);
        __builtin_amdgcn_s_setprio(0);
        STAMP(14);
        load_fb(0, 4);
        phase2<true>(smem, ws, wsr, x, out, fb, w, lane, ttile, ctile, [&]() INL { load_fb(4, NK); }, tag DBGARGS);
    } else {
        const int j = w & 1;
        if (w >= 6) {
#pragma unroll 1
            for (int t = 3; t < 10; ++t) {
                cnt_wait(cnt_ld(cD), cD, t - 2);
                stage_a(true);
                stage_b();
            }
            word_add(smem + M_S9, lane);
            cnt_wait(0, smem + M_F1, 1);
            load_fb(0, 1);
            asm volatile("s_waitcnt vmcnt(5)" ::: "memory");
            cnt_add(cR, (lane >> 5) == j);
            load_fb(1, NK);
            STAMPB(5);
        } else {
            DRAIN();
            u32 old = 0;
            if (lane == 0) old = __hip_atomic_fetch_add((LAS u32*)(smem + M_C4), 1u, __ATOMIC_RELAXED, __HIP_MEMORY_SCOPE_WORKGROUP);
            if (__builtin_amdgcn_readfirstlane(old) == 1 && lane < 2 && (lane == 0 || mainwg))
                st_flag(ws, WS_F4 + (lane ? fl * 10 + tt : 240 + sb) * 8, tag);
            STAMPB(8);
            auto turn = [&](u32 n) INL {
                cnt_wait<2>(0, cD, n);
                __builtin_amdgcn_s_sleep(PACE);
            };
            if (j == 0) {
                turn(1);
                wait_flags<4, NAP1>(ws, WS_F1 + lane * 8, 512, tag);
                word_add(smem + M_F1, lane);
                if (b == 0 && lane == 0) st_flag(ws, WS_EPOCH, tag);
                STAMPB(10);
                cnt_wait(0, cR, 1);
                cnt_wait(0, smem + M_C4, 2);
                if (lane < 30) {
#pragma unroll
                    for (int c = 0; c < 3; ++c) lds_st<u32x4>(smem + L_U + c * U_SZ + 4096 + lane * 16, u32x4{0, 0, 0, 0});
                }
                const u32 src = WS_XNT + ((lane >> 2) * 640 + ch0 - odd + min(p4, 2)) * 16;
                LAS char* const dst = lds_at(opaque(lds0 + L_U + min(p4, 2) * U_SZ + (15 - (lane >> 2)) * 16));
#pragma unroll
                for (int h = 0; h < 2; ++h) {
                    turn(3 + h);
                    u32x4 ug[8];
#pragma unroll
                    for (int i = 0; i < 8; ++i) ug[i] = ws_ld(wsr, src, (8 * h + i) * 16 * 640 * 16);
#pragma unroll
                    for (int i = 0; i < 8; ++i) lds_st<u32x4>(dst + (15 - 8 * h - i) * 256, ug[i]);
                }
            } else {
                const u32 nq = mainwg ? 16 : 24, lq = min((u32)lane, 2 * nq - 1), gu = lq >= nq;
                const u32 fcs = ((lane < 10 ? f0 : f0 + 4 * nq - 1) >> 6) * 10 + (lane < 10 ? lane : lane - 10);
                turn(2);
                wait_flags<1, NAP1>(ws, WS_F4 + (lane < 20 ? fcs : lane == 20 ? 240 + sb : 240 + (sb ^ 1)) * 8, 0, tag);
                cnt_wait<2>(0, smem + M_F1, 1);
                STAMPB(10);
                turn(5);
                u32x4 ex[13];
#pragma unroll
                for (int i = 0; i < 3; ++i) ex[10 + i] = ws_ld(wsr, WS_KP + (sb ^ 1) * PT_SZ + min((64 * i + lane) * 16, (int)PT_SZ - 16));
#pragma unroll
                for (int i = 0; i < 10; ++i) ex[i] = ws_ld(wsr, WS_CSP + gu * 6144 + (f0 + 4 * (lq - gu * nq)) * 4, i * 12288);
                const u32x4 st = ws_ld(wsr, WS_STAT + min(tok0 * 4 + min(lane, 47) * 16, 8176));
#pragma unroll
                for (int i = 0; i < 3; ++i) {
                    const u32 o = min((64 * i + lane) * 16, (int)PT_SZ - 16);
                    const f32x4 v = add4(lds_ld<f32x4>(smem + L_KS + o), as_f4(ex[10 + i]));
                    if ((64 * i + lane) * 16 < PT_SZ) lds_st<f32x4>(smem + L_KS + o, v);
                }
                f32x4 v = as_f4(ex[0]);
#pragma unroll
                for (int i = 1; i < 10; ++i) v = add4(v, as_f4(ex[i]));
                lds_st<f32x4>(smem + L_CS + lq * 16, v);
                lds_st<u32x4>(smem + L_STAT + min(lane, 47) * 16, st);
            }
            word_add(smem + M_SD, lane);
            cnt_wait<2>(0, smem + M_SD, 2);
            {
                float g[2][9];
                u32 gb2, gs2;
                lag_slots(chunk + 128, gb2, gs2);
                get_lags(g[0], smem + L_KS, gbase, gsteps);
                get_lags(g[1], smem + L_KS, gb2, gs2);
                const float first = lds_ld<float>(smem + L_KS);
                word_add(smem + M_KR, lane);
                cnt_wait(0, smem + M_KR, 2);
#pragma unroll
                for (int h = 0; h < 2; ++h) put_images(smem + L_K + 4 * K_SZ, chunk + 128 * h, g[h], first);
                word_add(smem + M_KB, lane);
            }
            STAMPB(11);
            turn(6);
            load_fb(0, NEARLY);
            cnt_wait<2>(0, smem + M_S9, 2);
            load_fb(NEARLY, NK);
            STAMPB(5);
        }
        cnt_wait<2>(0, smem + M_KB, 2);
        STAMP(21);
        const int a = lane & 15;
        auto kbase = [&](int c) INL { return lds0 + L_K + (2 * c + 1 - (a & 1)) * K_SZ + ((8 * g4 + a + 1) >> 1) * 4; };
        auto ubase = [&](int c) INL { return lds0 + L_U + (c == 2 ? 2 - 2 * odd : c + odd) * U_SZ + (2032 - 16 * a + 8 * g4) * 2 - 7 * 512; };
        f32x4 y[8];
#pragma unroll
        for (int t = 0; t < 8; ++t) y[t] = f32x4{0.f, 0.f, 0.f, 0.f};
        auto ldk4 = [&](const u32 (&kb)[4], int off) INL {
            LAS char* const k = lds_at(kb[off >> 10]) + (off & 1023);
            return u32x4{lds_ld<u32>(k), lds_ld<u32>(k + 4), lds_ld<u32>(k + 8), lds_ld<u32>(k + 12)};
        };
        auto go = [&]() INL {
#pragma unroll
            for (int t = 0; t < 8; ++t) asm volatile("" : "+v"(y[t]));
            cnt_wait<2>(0, smem + M_EP, 4);
            __builtin_amdgcn_s_setprio(1);
            STAMP(12);
            SB();
        };
        auto tri = [&](auto nt, u32 k0, u32 u0, u32 kstep, u32 ustep, int npair, bool flush) INL {
            constexpr int NT = decltype(nt)::value, P = NT * (NT + 1);
            typedef ConvPlan<NT> plan;
            u32 kb[4], kn[4], up = opaque(u0), un;
#pragma unroll
            for (int i = 0; i < 4; ++i) kb[i] = opaque(k0 + 1024 * i);
            u32x4 uf[NT];
            u32x2 kf[2 * NT][2];
            auto ldk = [&](const u32 (&kq)[4], int q) INL {
                const int n = q >> 1, off = (n < NT ? n * 512 : (2 * NT - 1 - n) * 512 + 64) + 8 * (q & 1);
                LAS char* const k = lds_at(kq[off >> 10]) + (off & 1023);
                kf[n][q & 1] = u32x2{lds_ld<u32>(k), lds_ld<u32>(k + 4)};
            };
#pragma unroll
            for (int t = 0; t < NT; ++t) if (plan::u2[t] < P) uf[t] = lds_ld<u32x4>(lds_at(up) + (7 - t) * 512);
#pragma unroll
            for (int q = 0; q < 4 * NT; ++q) if (plan::k[q] < 0) ldk(kb, q);
            go();
#pragma unroll 1
            for (int it = 0; it < npair; ++it) {
                int p = 0;
#pragma unroll
                for (int n = 0; n < 2 * NT; ++n) {
                    const int cnt = n < NT ? NT - n : n - NT + 1;
#pragma unroll
                    for (int i = 0; i < cnt; ++i, ++p) {
                        const int tau = n == 2 * NT - 1 ? NT - 1 - i : i, sg = n < NT ? n : 2 * NT - 1 - n, reg = n < NT ? tau : NT - 1 - tau;
                        y[sg + tau] = mma16(u32x4{kf[n][0].x, kf[n][0].y, kf[n][1].x, kf[n][1].y}, uf[reg], y[sg + tau]);
                        SB();
#pragma unroll
                        for (int q = 0; q < 4 * NT; ++q) {
                            if (plan::k[q] == p) ldk(kb, q);
                            if (plan::k[q] + P == p) ldk(kn, q);
                        }
#pragma unroll
                        for (int q = 0; q < NT; ++q) {
                            if (plan::u1[q] == p) uf[NT - 1 - q] = lds_ld<u32x4>(lds_at(up) + (7 - q) * 512 + 64);
                            if (plan::u2[q] == p) uf[q] = lds_ld<u32x4>(lds_at(un) + (7 - q) * 512);
                            if (plan::u2[q] - P == p) uf[q] = lds_ld<u32x4>(lds_at(up) + (7 - q) * 512);
                        }
                        if (p == 4) { un = opaque(up + ustep); kn[0] = opaque(kb[0] + kstep); }
                        if (p == 5) { kn[1] = opaque(kb[1] + kstep); kn[2] = opaque(kb[2] + kstep); }
                        if (p == 14) kn[3] = opaque(kb[3] + kstep);
                        SB();
                    }
                }
                up = un;
#pragma unroll
                for (int i = 0; i < 4; ++i) kb[i] = kn[i];
                if (flush) {
                    cnt_wait(0, cD, 9);
#pragma unroll
                    for (int t = 0; t < NT; ++t) {
                        lds_st<f32x4>(smem + L_CP + it * 8192 + t * 1024 + lane * 16, y[t]);
                        y[t] = f32x4{0.f, 0.f, 0.f, 0.f};
                    }
                    word_add(smem + M_CF + it * 4, lane);
                }
            }
        };
        if (w4 < 3) {
            const bool help = w4 == 2;
            const int c = help ? 0 : w4;
            tri(std::integral_constant<int, 8>{}, kbase(c) + (help ? 384 : 0), ubase(c) + (help ? 384 : 0), help ? 2 * K_SZ : 128, help ? U_SZ : 128, help ? 2 : 3, help);
        } else if (!odd) {
            tri(std::integral_constant<int, 6>{}, kbase(2), ubase(2), 128, 128, 4, false);
        } else {
            u32 kb[4], up = opaque(ubase(2));
#pragma unroll
            for (int i = 0; i < 4; ++i) kb[i] = opaque(kbase(2) + 1024 * i);
            go();
#pragma unroll 1
            for (int rho = 0; rho < 8; ++rho) {
                u32x4 kf[8], uf[8];
#pragma unroll
                for (int n = 0; n < 8; ++n) { uf[n] = lds_ld<u32x4>(lds_at(up) + n * 512); kf[n] = ldk4(kb, n * 512); }
                SB();
#pragma unroll
                for (int n = 0; n < 8; ++n) {
                    y[7] = mma16(kf[n], uf[n], y[7]);
                    if (n < 7) y[6] = mma16(kf[n], uf[n + 1], y[6]);
                }
                up = opaque(up + 64);
#pragma unroll
                for (int i = 0; i < 4; ++i) kb[i] = opaque(kb[i] + 64);
            }
        }
        STAMP(13);
        if (w4 < 2) {
            cnt_wait(0, smem + M_CF + w4 * 4, 1);
#pragma unroll
            for (int t = 0; t < 8; ++t) y[t] += lds_ld<f32x4>(smem + L_CP + w4 * 8192 + t * 1024 + lane * 16);
        }
        if (w4 != 2) {
            const u32 off = WS_YT + (w4 == 3 ? chs : ch0 + w4) * 4096 + (g4 & 1) * 512 + (16 * a + 8 * (g4 >> 1)) * 2;
#pragma unroll
            for (int t = 0; t < 8; t += 2) {
                if (w4 < 2 || (t >= 6) == (odd != 0)) {
                    const auto lo = __builtin_amdgcn_permlane16_swap(pk_bf(y[t][0], y[t][1]), pk_bf(y[t + 1][0], y[t + 1][1]), false, false);
                    const auto hi = __builtin_amdgcn_permlane16_swap(pk_bf(y[t][2], y[t][3]), pk_bf(y[t + 1][2], y[t + 1][3]), false, false);
                    ws_st(wsr, off + t * 512, u32x4{lo[0], hi[0], lo[1], hi[1]});
                }
            }
            word_add(smem + M_YS, lane);
        }
        auto publish_y = [&]() INL { if (w4 != 2) signal_last(smem + M_C3, 3, ws, WS_F3 + sb * 8, tag, lane); };
        __builtin_amdgcn_s_setprio(0);
        STAMP(14);
        phase2<false>(smem, ws, wsr, x, out, fb, w, lane, ttile, ctile, publish_y, tag DBGARGS);
    }
#endif
}

extern "C" void kernel_launch(void* const* d_in, const int* in_sizes, int n_in, void* d_out, int out_size, void* d_ws, size_t ws_size,
                              hipStream_t stream) {
    (void)hipFuncSetAttribute((const void*)fused_kernel, hipFuncAttributeMaxDynamicSharedMemorySize, LDS_TOTAL);
    fused_kernel<<<256, 512, LDS_TOTAL, stream>>>((const u16*)d_in[0], (const u16*)d_in[1], (const u16*)d_in[2], (const u16*)d_in[3],
                                                  (const u16*)d_in[4], (const u16*)d_in[5], (const u16*)d_in[6], (const u16*)d_in[7],
                                                  (const u16*)d_in[8], (const u16*)d_in[10], (const u16*)d_in[11], (const u16*)d_in[12],
                                                  (u16*)d_out, (char*)d_ws);
}
```
